```python
import torch

def _softmax(x, axis=-1):
    return torch.softmax(x, dim=axis)

def _gelu(x):
    return torch.nn.functional.gelu(x, approximate='tanh')

def _silu(x):
    return torch.nn.functional.silu(x)

def _sigmoid(x):
    return torch.sigmoid(x)

def _layernorm(x, axis=-1, eps=1e-05):
    m = x.mean(dim=axis, keepdim=True)
    v = x.var(dim=axis, keepdim=True, unbiased=False)
    return (x - m) / torch.sqrt(v + eps)

def _scatter_add(tgt, idx, src, axis=0):
    return tgt.clone().index_add_(axis, idx, src)

def _segment_sum(src, seg, n):
    out = torch.zeros((n,) + src.shape[1:], dtype=src.dtype)
    return out.index_add_(0, seg, src)

def _pad(x, pw):
    flat = tuple((v for pair in reversed(pw) for v in pair))
    return torch.nn.functional.pad(x, flat)
kernel_name = 'standalone__varlen_attn_packed__ragged'

def setup_inputs():

    _w = lambda a: (lambda t: t.to(torch.bfloat16) if t.is_floating_point() else t)(torch.as_tensor(a))
    cu = torch.tensor([0, 321, 555, 878, 1299, 1551, 1670, 1878, 2181, 2351, 2646, 2810, 3248, 3471, 3811, 4169, 4391], dtype=torch.int64)
    T, H, D = (int(cu[-1]), 16, 128)
    return {'q': _w((lambda *s: torch.randn(*s, dtype=torch.bfloat16) * 0.8)(T, H, D)), 'k': _w((lambda *s: torch.randn(*s, dtype=torch.bfloat16) * 0.8)(T, H, D)), 'v': _w((lambda *s: torch.randn(*s, dtype=torch.bfloat16) * 0.05)(T, H, D)), 'cu': _w(cu)}

def reference(q, k, v, cu):
    T, H, D = q.shape
    out = torch.zeros_like(q)
    scale = D ** (-0.5)
    outs = []
    for i in range(len(cu) - 1):
        s, e = (int(cu[i]), int(cu[i + 1]))
        qi, ki, vi = (q[s:e].clone(), k[s:e].clone(), v[s:e].clone())
        p = _softmax(torch.einsum('qhd,khd->hqk', qi, ki) * scale, axis=-1)
        outs.append(torch.einsum('hqk,khd->qhd', p, vi))
    return torch.concatenate(outs, axis=0)
```

```cpp
#include <hip/hip_runtime.h>
#include <hip/hip_bf16.h>

typedef float    f32x16 __attribute__((ext_vector_type(16)));
typedef float    f32x2  __attribute__((ext_vector_type(2)));
typedef __bf16   bf16x8 __attribute__((ext_vector_type(8)));
typedef __bf16   bf16x2 __attribute__((ext_vector_type(2)));
typedef short    s16x4  __attribute__((ext_vector_type(4)));
typedef short    s16x8  __attribute__((ext_vector_type(8)));
typedef unsigned u32x4  __attribute__((ext_vector_type(4)));
typedef unsigned u32x2  __attribute__((ext_vector_type(2)));
typedef __attribute__((address_space(3))) char* lds_ptr;
#define DI __device__ __forceinline__

constexpr int H = 16, D = 128, ROWB = H * D * 2;
constexpr int STAGE = 32768;

DI unsigned rfl(unsigned x) { return __builtin_amdgcn_readfirstlane(x); }
DI unsigned cvtpk(float lo, float hi) { f32x2 v = {lo, hi}; bf16x2 b = __builtin_convertvector(v, bf16x2); return __builtin_bit_cast(unsigned, b); }
DI void dma16(unsigned m0v, unsigned voff, __amdgpu_buffer_rsrc_t srd, unsigned soff) {
  asm volatile("s_mov_b32 m0, %0\n\ts_nop 0\n\tbuffer_load_dwordx4 %1, %2, %3 offen lds" :: "s"(m0v), "v"(voff), "s"(srd), "s"(soff) : "memory");
}
DI s16x4 vtr(lds_ptr p) { return __builtin_amdgcn_ds_read_tr16_b64_v4i16((__attribute__((address_space(3))) s16x4*)p); }

__global__ __launch_bounds__(256, 2)
void attn_v1(const unsigned short* __restrict__ q, const unsigned short* __restrict__ k, const unsigned short* __restrict__ v,
             const int* __restrict__ cu, unsigned short* __restrict__ o, int nseq) {
  __shared__ __attribute__((aligned(16))) char smem[2 * STAGE];
  const int b = blockIdx.x, x = b & 7, i = b >> 3, hh = i & 1; int j = i >> 1;
  int s0 = 0, L = 0, seq = -1;
  for (int s = 0; s < nseq; ++s) {
    const int a = cu[s], e = cu[s + 1], n = (e - a + 127) >> 7;
    if (seq < 0) { if (j < n) { seq = s; s0 = a; L = e - a; } else j -= n; }
  }
  if (seq < 0) return;
  const int h = ((x + seq) & 7) + 8 * hh;
  const int tid = threadIdx.x, lane = tid & 63, r32 = lane & 31, hi = lane >> 5; const int wid = rfl(tid >> 6);
  const size_t base = ((size_t)s0 * H + h) * D;
  const int nrec = (L - 1) * ROWB + 256;
  const __amdgpu_buffer_rsrc_t srdQ = __builtin_amdgcn_make_buffer_rsrc((void*)(q + base), (short)0, nrec, 0x00020000);
  const __amdgpu_buffer_rsrc_t srdK = __builtin_amdgcn_make_buffer_rsrc((void*)(k + base), (short)0, nrec, 0x00020000);
  const __amdgpu_buffer_rsrc_t srdV = __builtin_amdgcn_make_buffer_rsrc((void*)(v + base), (short)0, nrec, 0x00020000);
  const __amdgpu_buffer_rsrc_t srdO = __builtin_amdgcn_make_buffer_rsrc((void*)(o + base), (short)0, nrec, 0x00020000);
  const unsigned lds0 = (unsigned)(size_t)(lds_ptr)smem;
  const unsigned ksrc = ((lane >> 2) & 7) * ROWB + hi * 64 + (((lane & 3) ^ (2 * (wid & 1) + ((lane >> 4) & 1))) * 16);
  const unsigned vsrc = ((lane >> 2) & 7) * ROWB + hi * 64 + (lane & 3) * 16;
  auto stage = [&](int t) {
    const unsigned dst = lds0 + (t & 1) * STAGE + wid * 2048, so = (unsigned)(t * 64 + wid * 8) * ROWB;
#pragma unroll
    for (int g = 0; g < 2; ++g)
#pragma unroll
      for (int p = 0; p < 2; ++p) dma16(dst + g * 8192 + p * 1024, ksrc, srdK, so + g * 32 * ROWB + p * 128);
#pragma unroll
    for (int g = 0; g < 2; ++g)
#pragma unroll
      for (int p = 0; p < 2; ++p) dma16(dst + 16384 + g * 8192 + p * 1024, vsrc, srdV, so + g * 32 * ROWB + p * 128);
  };
  const int NT = (L + 63) >> 6;
  stage(0);
  const int q0 = j * 128 + wid * 32;
  bf16x8 qr[8];
#pragma unroll
  for (int s = 0; s < 8; ++s) qr[s] = __builtin_bit_cast(bf16x8, __builtin_amdgcn_raw_buffer_load_b128(srdQ, (q0 + r32) * ROWB + hi * 16 + s * 32, 0, 0));
  const unsigned kb0 = 2048 * (r32 >> 3) + 64 * (r32 & 7) + 16 * ((0 + hi) ^ ((r32 >> 2) & 3));
  const unsigned kb1 = 2048 * (r32 >> 3) + 64 * (r32 & 7) + 16 * ((2 + hi) ^ ((r32 >> 2) & 3));
  const unsigned vb = 16384 + 64 * (4 * hi + ((lane >> 2) & 3)) + 32 * ((lane >> 4) & 1) + 8 * (lane & 3);
  constexpr float C = 0.12751743082459868f;
  float m = -1e30f, l = 0.f;
  f32x16 oacc[4] = {};
  for (int t = 0; t < NT; ++t) {
    asm volatile("s_waitcnt vmcnt(0) lgkmcnt(0)\n\ts_barrier" ::: "memory");
    if (t + 1 < NT) stage(t + 1);
    lds_ptr sb = (lds_ptr)smem + (t & 1) * STAGE;
    f32x16 p0 = {}, p1 = {};
#pragma unroll
    for (int s = 0; s < 8; ++s) {
      const unsigned a = ((s & 1) ? kb1 : kb0) + 512 * (s >> 1);
      const bf16x8 k0 = *(const __attribute__((address_space(3))) bf16x8*)(sb + a);
      const bf16x8 k1 = *(const __attribute__((address_space(3))) bf16x8*)(sb + a + 8192);
      p0 = __builtin_amdgcn_mfma_f32_32x32x16_bf16(k0, qr[s], p0, 0, 0, 0);
      p1 = __builtin_amdgcn_mfma_f32_32x32x16_bf16(k1, qr[s], p1, 0, 0, 0);
    }
    if (t == NT - 1) {
      const int lim = L - t * 64 - 4 * hi;
#pragma unroll
      for (int r = 0; r < 16; ++r) { const int kk = (r & 3) + 8 * (r >> 2); if (kk >= lim) p0[r] = -INFINITY; if (kk + 32 >= lim) p1[r] = -INFINITY; }
    }
    float pm = fmaxf(p0[0], p1[0]);
#pragma unroll
    for (int r = 1; r < 16; ++r) pm = fmaxf(fmaxf(pm, p0[r]), p1[r]);
    { auto rr = __builtin_amdgcn_permlane32_swap(__float_as_uint(pm), __float_as_uint(pm), false, false); pm = fmaxf(__uint_as_float(rr[0]), __uint_as_float(rr[1])); }
    if (__any(pm > m)) {
      const float mn = fmaxf(m, pm), al = __builtin_amdgcn_exp2f((m - mn) * C);
      m = mn; l *= al;
#pragma unroll
      for (int d = 0; d < 4; ++d) oacc[d] *= al;
    }
    const float nm = -m * C;
    float ps = 0.f;
#pragma unroll
    for (int r = 0; r < 16; ++r) { p0[r] = __builtin_amdgcn_exp2f(fmaf(p0[r], C, nm)); p1[r] = __builtin_amdgcn_exp2f(fmaf(p1[r], C, nm)); ps += p0[r] + p1[r]; }
    l += ps;
    u32x4 pw[4];
#pragma unroll
    for (int e = 0; e < 4; ++e) { pw[0][e] = cvtpk(p0[2 * e], p0[2 * e + 1]); pw[1][e] = cvtpk(p0[8 + 2 * e], p0[9 + 2 * e]); pw[2][e] = cvtpk(p1[2 * e], p1[2 * e + 1]); pw[3][e] = cvtpk(p1[8 + 2 * e], p1[9 + 2 * e]); }
#pragma unroll
    for (int ks = 0; ks < 4; ++ks)
#pragma unroll
      for (int d = 0; d < 4; ++d) {
        const s16x4 lo = vtr(sb + vb + 4096 * ks + 512 * d), up = vtr(sb + vb + 4096 * ks + 2048 + 512 * d);
        const s16x8 vf = __builtin_shufflevector(lo, up, 0, 1, 2, 3, 4, 5, 6, 7);
        oacc[d] = __builtin_amdgcn_mfma_f32_32x32x16_bf16(__builtin_bit_cast(bf16x8, vf), __builtin_bit_cast(bf16x8, pw[ks]), oacc[d], 0, 0, 0);
      }
  }
  { auto rr = __builtin_amdgcn_permlane32_swap(__float_as_uint(l), __float_as_uint(l), false, false); l = __uint_as_float(rr[0]) + __uint_as_float(rr[1]); }
  const float il = __builtin_amdgcn_rcpf(l);
  asm volatile("s_waitcnt lgkmcnt(0)\n\ts_barrier" ::: "memory");
  lds_ptr st = (lds_ptr)smem + wid * 8192;
#pragma unroll
  for (int d = 0; d < 4; ++d)
#pragma unroll
    for (int g = 0; g < 4; ++g) {
      u32x2 w = {cvtpk(oacc[d][4 * g] * il, oacc[d][4 * g + 1] * il), cvtpk(oacc[d][4 * g + 2] * il, oacc[d][4 * g + 3] * il)};
      *(__attribute__((address_space(3))) u32x2*)(st + 256 * r32 + 16 * ((4 * d + g) ^ (r32 & 15)) + 8 * hi) = w;
    }
  asm volatile("s_waitcnt lgkmcnt(0)" ::: "memory");
#pragma unroll
  for (int ii = 0; ii < 8; ++ii) {
    const int r = 4 * ii + (lane >> 4), c = lane & 15;
    const u32x4 w = *(const __attribute__((address_space(3))) u32x4*)(st + 256 * r + 16 * (c ^ (r & 15)));
    __builtin_amdgcn_raw_buffer_store_b128(w, srdO, (q0 + r) * ROWB + c * 16, 0, 0);
  }
}

extern "C" void kernel_launch(void* const* d_in, const int* in_sizes, int n_in,
                              void* d_out, int out_size, void* d_ws, size_t ws_size,
                              hipStream_t stream) {
  const int T = in_sizes[0] / (H * D), nseq = in_sizes[3] - 1;
  const int ni = T / 128 + nseq;
  attn_v1<<<ni * 16, 256, 0, stream>>>((const unsigned short*)d_in[0], (const unsigned short*)d_in[1], (const unsigned short*)d_in[2],
                                       (const int*)d_in[3], (unsigned short*)d_out, nseq);
}
```

```cpp
#include <hip/hip_runtime.h>
#include <hip/hip_bf16.h>

typedef float    f32x16 __attribute__((ext_vector_type(16)));
typedef float    f32x2  __attribute__((ext_vector_type(2)));
typedef __bf16   bf16x8 __attribute__((ext_vector_type(8)));
typedef __bf16   bf16x2 __attribute__((ext_vector_type(2)));
typedef short    s16x4  __attribute__((ext_vector_type(4)));
typedef short    s16x8  __attribute__((ext_vector_type(8)));
typedef unsigned u32x4  __attribute__((ext_vector_type(4)));
typedef unsigned u32x2  __attribute__((ext_vector_type(2)));
typedef __attribute__((address_space(3))) char* lds_ptr;
#define DI __device__ __forceinline__

constexpr int H = 16, D = 128, ROWB = H * D * 2;
constexpr int STAGE = 32768;

DI unsigned rfl(unsigned x) { return __builtin_amdgcn_readfirstlane(x); }
DI unsigned cvtpk(float lo, float hi) { f32x2 v = {lo, hi}; bf16x2 b = __builtin_convertvector(v, bf16x2); return __builtin_bit_cast(unsigned, b); }
DI void dma16(unsigned m0v, unsigned voff, __amdgpu_buffer_rsrc_t srd, unsigned soff) {
  asm volatile("s_mov_b32 m0, %0\n\ts_nop 0\n\tbuffer_load_dwordx4 %1, %2, %3 offen lds" :: "s"(m0v), "v"(voff), "s"(srd), "s"(soff) : "memory");
}
DI s16x4 vtr(lds_ptr p) { return __builtin_amdgcn_ds_read_tr16_b64_v4i16((__attribute__((address_space(3))) s16x4*)p); }

__global__ __launch_bounds__(256, 2)
void attn_v1(const unsigned short* __restrict__ q, const unsigned short* __restrict__ k, const unsigned short* __restrict__ v,
             const int* __restrict__ cu, unsigned short* __restrict__ o, int nseq, unsigned long long* stp) {
  const unsigned long long T0 = __builtin_amdgcn_s_memrealtime(), C0 = __builtin_amdgcn_s_memtime(); unsigned long long T2 = 0;
  __shared__ __attribute__((aligned(16))) char smem[2 * STAGE];
  const int b = blockIdx.x, x = b & 7, i = b >> 3, hh = i & 1; int j = i >> 1;
  int s0 = 0, L = 0, seq = -1;
  for (int s = 0; s < nseq; ++s) {
    const int a = cu[s], e = cu[s + 1], n = (e - a + 127) >> 7;
    if (seq < 0) { if (j < n) { seq = s; s0 = a; L = e - a; } else j -= n; }
  }
  if (seq < 0) { if (threadIdx.x == 0) stp[blockIdx.x * 8] = 0; return; }
  const unsigned long long T1 = __builtin_amdgcn_s_memrealtime();
  const int h = ((x + seq) & 7) + 8 * hh;
  const int tid = threadIdx.x, lane = tid & 63, r32 = lane & 31, hi = lane >> 5; const int wid = rfl(tid >> 6);
  const size_t base = ((size_t)s0 * H + h) * D;
  const int nrec = (L - 1) * ROWB + 256;
  const __amdgpu_buffer_rsrc_t srdQ = __builtin_amdgcn_make_buffer_rsrc((void*)(q + base), (short)0, nrec, 0x00020000);
  const __amdgpu_buffer_rsrc_t srdK = __builtin_amdgcn_make_buffer_rsrc((void*)(k + base), (short)0, nrec, 0x00020000);
  const __amdgpu_buffer_rsrc_t srdV = __builtin_amdgcn_make_buffer_rsrc((void*)(v + base), (short)0, nrec, 0x00020000);
  const __amdgpu_buffer_rsrc_t srdO = __builtin_amdgcn_make_buffer_rsrc((void*)(o + base), (short)0, nrec, 0x00020000);
  const unsigned lds0 = (unsigned)(size_t)(lds_ptr)smem;
  const unsigned ksrc = ((lane >> 2) & 7) * ROWB + hi * 64 + (((lane & 3) ^ (2 * (wid & 1) + ((lane >> 4) & 1))) * 16);
  const unsigned vsrc = ((lane >> 2) & 7) * ROWB + hi * 64 + (lane & 3) * 16;
  auto stage = [&](int t) {
    const unsigned dst = lds0 + (t & 1) * STAGE + wid * 2048, so = (unsigned)(t * 64 + wid * 8) * ROWB;
#pragma unroll
    for (int g = 0; g < 2; ++g)
#pragma unroll
      for (int p = 0; p < 2; ++p) dma16(dst + g * 8192 + p * 1024, ksrc, srdK, so + g * 32 * ROWB + p * 128);
#pragma unroll
    for (int g = 0; g < 2; ++g)
#pragma unroll
      for (int p = 0; p < 2; ++p) dma16(dst + 16384 + g * 8192 + p * 1024, vsrc, srdV, so + g * 32 * ROWB + p * 128);
  };
  const int NT = (L + 63) >> 6;
  stage(0);
  const int q0 = j * 128 + wid * 32;
  bf16x8 qr[8];
#pragma unroll
  for (int s = 0; s < 8; ++s) qr[s] = __builtin_bit_cast(bf16x8, __builtin_amdgcn_raw_buffer_load_b128(srdQ, (q0 + r32) * ROWB + hi * 16 + s * 32, 0, 0));
  const unsigned kb0 = 2048 * (r32 >> 3) + 64 * (r32 & 7) + 16 * ((0 + hi) ^ ((r32 >> 2) & 3));
  const unsigned kb1 = 2048 * (r32 >> 3) + 64 * (r32 & 7) + 16 * ((2 + hi) ^ ((r32 >> 2) & 3));
  const unsigned vb = 16384 + 64 * (4 * hi + ((lane >> 2) & 3)) + 32 * ((lane >> 4) & 1) + 8 * (lane & 3);
  constexpr float C = 0.12751743082459868f;
  float m = -1e30f, l = 0.f;
  f32x16 oacc[4] = {};
  for (int t = 0; t < NT; ++t) {
    asm volatile("s_waitcnt vmcnt(0) lgkmcnt(0)\n\ts_barrier" ::: "memory");
    if (t == 0) T2 = __builtin_amdgcn_s_memrealtime();
    if (t + 1 < NT) stage(t + 1);
    lds_ptr sb = (lds_ptr)smem + (t & 1) * STAGE;
    f32x16 p0 = {}, p1 = {};
#pragma unroll
    for (int s = 0; s < 8; ++s) {
      const unsigned a = ((s & 1) ? kb1 : kb0) + 512 * (s >> 1);
      const bf16x8 k0 = *(const __attribute__((address_space(3))) bf16x8*)(sb + a);
      const bf16x8 k1 = *(const __attribute__((address_space(3))) bf16x8*)(sb + a + 8192);
      p0 = __builtin_amdgcn_mfma_f32_32x32x16_bf16(k0, qr[s], p0, 0, 0, 0);
      p1 = __builtin_amdgcn_mfma_f32_32x32x16_bf16(k1, qr[s], p1, 0, 0, 0);
    }
    if (t == NT - 1) {
      const int lim = L - t * 64 - 4 * hi;
#pragma unroll
      for (int r = 0; r < 16; ++r) { const int kk = (r & 3) + 8 * (r >> 2); if (kk >= lim) p0[r] = -INFINITY; if (kk + 32 >= lim) p1[r] = -INFINITY; }
    }
    float pm = fmaxf(p0[0], p1[0]);
#pragma unroll
    for (int r = 1; r < 16; ++r) pm = fmaxf(fmaxf(pm, p0[r]), p1[r]);
    { auto rr = __builtin_amdgcn_permlane32_swap(__float_as_uint(pm), __float_as_uint(pm), false, false); pm = fmaxf(__uint_as_float(rr[0]), __uint_as_float(rr[1])); }
    if (__any(pm > m)) {
      const float mn = fmaxf(m, pm), al = __builtin_amdgcn_exp2f((m - mn) * C);
      m = mn; l *= al;
#pragma unroll
      for (int d = 0; d < 4; ++d) oacc[d] *= al;
    }
    const float nm = -m * C;
    float ps = 0.f;
#pragma unroll
    for (int r = 0; r < 16; ++r) { p0[r] = __builtin_amdgcn_exp2f(fmaf(p0[r], C, nm)); p1[r] = __builtin_amdgcn_exp2f(fmaf(p1[r], C, nm)); ps += p0[r] + p1[r]; }
    l += ps;
    u32x4 pw[4];
#pragma unroll
    for (int e = 0; e < 4; ++e) { pw[0][e] = cvtpk(p0[2 * e], p0[2 * e + 1]); pw[1][e] = cvtpk(p0[8 + 2 * e], p0[9 + 2 * e]); pw[2][e] = cvtpk(p1[2 * e], p1[2 * e + 1]); pw[3][e] = cvtpk(p1[8 + 2 * e], p1[9 + 2 * e]); }
#pragma unroll
    for (int ks = 0; ks < 4; ++ks)
#pragma unroll
      for (int d = 0; d < 4; ++d) {
        const s16x4 lo = vtr(sb + vb + 4096 * ks + 512 * d), up = vtr(sb + vb + 4096 * ks + 2048 + 512 * d);
        const s16x8 vf = __builtin_shufflevector(lo, up, 0, 1, 2, 3, 4, 5, 6, 7);
        oacc[d] = __builtin_amdgcn_mfma_f32_32x32x16_bf16(__builtin_bit_cast(bf16x8, vf), __builtin_bit_cast(bf16x8, pw[ks]), oacc[d], 0, 0, 0);
      }
  }
  { auto rr = __builtin_amdgcn_permlane32_swap(__float_as_uint(l), __float_as_uint(l), false, false); l = __uint_as_float(rr[0]) + __uint_as_float(rr[1]); }
  const float il = __builtin_amdgcn_rcpf(l);
  asm volatile("s_waitcnt lgkmcnt(0)\n\ts_barrier" ::: "memory");
  lds_ptr st = (lds_ptr)smem + wid * 8192;
#pragma unroll
  for (int d = 0; d < 4; ++d)
#pragma unroll
    for (int g = 0; g < 4; ++g) {
      u32x2 w = {cvtpk(oacc[d][4 * g] * il, oacc[d][4 * g + 1] * il), cvtpk(oacc[d][4 * g + 2] * il, oacc[d][4 * g + 3] * il)};
      *(__attribute__((address_space(3))) u32x2*)(st + 256 * r32 + 16 * ((4 * d + g) ^ (r32 & 15)) + 8 * hi) = w;
    }
  asm volatile("s_waitcnt lgkmcnt(0)" ::: "memory");
#pragma unroll
  for (int ii = 0; ii < 8; ++ii) {
    const int r = 4 * ii + (lane >> 4), c = lane & 15;
    const u32x4 w = *(const __attribute__((address_space(3))) u32x4*)(st + 256 * r + 16 * (c ^ (r & 15)));
    __builtin_amdgcn_raw_buffer_store_b128(w, srdO, (q0 + r) * ROWB + c * 16, 0, 0);
  }
  asm volatile("s_waitcnt vmcnt(0)" ::: "memory");
  if (threadIdx.x == 0) { unsigned long long* p = stp + blockIdx.x * 8; p[0] = T0; p[1] = T1; p[2] = T2; p[3] = __builtin_amdgcn_s_memrealtime(); p[4] = __builtin_amdgcn_s_memtime() - C0; }
}


template <int MODE> __global__ void k_amp(const unsigned long long* st, int n, int K) {
  unsigned long long t0 = __builtin_amdgcn_s_memrealtime();
  __shared__ unsigned long long a, b, a2, s1, s2, sc, sr, c;
  if (threadIdx.x == 0) { a = ~0ull; b = a2 = s1 = s2 = sc = sr = c = 0; }
  __syncthreads();
  const int i = threadIdx.x;
  if (i < n) { const unsigned long long* p = st + 8 * i; unsigned long long x = p[0];
    if (x != 0) { atomicMin(&a, x); atomicMax(&b, p[3]); atomicAdd(&sc, p[4]); atomicAdd(&sr, p[3] - p[0]);
     if (i < 512) { atomicMax(&a2, x); atomicAdd(&s1, p[1] - p[0]); atomicAdd(&s2, p[2] - p[0]); atomicAdd(&c, 1ull); } } }
  __syncthreads();
  if (threadIdx.x != 0) return;
  unsigned long long d = MODE == 0 ? b - a : MODE == 1 ? sc * 100 / sr : MODE == 2 ? s2 / c : MODE == 3 ? a2 - a : MODE == 4 ? s1 / c : t0 - b;
  if (d > 20000) d = 20000;
  while (__builtin_amdgcn_s_memrealtime() - t0 < d * K + 10000) __builtin_amdgcn_s_sleep(8);
}

extern "C" void kernel_launch(void* const* d_in, const int* in_sizes, int n_in,
                              void* d_out, int out_size, void* d_ws, size_t ws_size,
                              hipStream_t stream) {
  const int T = in_sizes[0] / (H * D), nseq = in_sizes[3] - 1;
  const int ni = T / 128 + nseq;
  attn_v1<<<ni * 16, 256, 0, stream>>>((const unsigned short*)d_in[0], (const unsigned short*)d_in[1], (const unsigned short*)d_in[2],
                                       (const int*)d_in[3], (unsigned short*)d_out, nseq, (unsigned long long*)d_ws);
  k_amp<3><<<1, 1024, 0, stream>>>((const unsigned long long*)d_ws, ni * 16, 100);
}
```

```cpp
#include <hip/hip_runtime.h>
#include <hip/hip_bf16.h>

typedef float    f32x16 __attribute__((ext_vector_type(16)));
typedef float    f32x2  __attribute__((ext_vector_type(2)));
typedef __bf16   bf16x8 __attribute__((ext_vector_type(8)));
typedef __bf16   bf16x2 __attribute__((ext_vector_type(2)));
typedef short    s16x4  __attribute__((ext_vector_type(4)));
typedef short    s16x8  __attribute__((ext_vector_type(8)));
typedef unsigned u32x4  __attribute__((ext_vector_type(4)));
typedef unsigned u32x2  __attribute__((ext_vector_type(2)));
typedef __attribute__((address_space(3))) char* lds_ptr;
#define DI __device__ __forceinline__

constexpr int H = 16, D = 128, ROWB = H * D * 2, WGS = 2, GRID = 256 * WGS;

DI unsigned rfl(unsigned x) { return __builtin_amdgcn_readfirstlane(x); }
DI unsigned cvtpk(float lo, float hi) { f32x2 v = {lo, hi}; bf16x2 b = __builtin_convertvector(v, bf16x2); return __builtin_bit_cast(unsigned, b); }
DI s16x4 vtr(lds_ptr p) { return __builtin_amdgcn_ds_read_tr16_b64_v4i16((__attribute__((address_space(3))) s16x4*)p); }


struct Args { const unsigned short *q, *k, *v; const int* cu; unsigned short* o; int nseq, qstep; };

__global__ __launch_bounds__(256, WGS)
void attn_varlen(const unsigned short* __restrict__ q, const unsigned short* __restrict__ k, const unsigned short* __restrict__ v,
                 const int* __restrict__ cu, unsigned short* __restrict__ o, int nseq, int qstep) {
  __shared__ __attribute__((aligned(16))) char smem[81920];
  typedef const __attribute__((address_space(4))) Args* argp;
  const unsigned b = blockIdx.x, wid = rfl(threadIdx.x >> 6);
  unsigned g0 = 0, rd = b >> 8;
  for (;;) {
    {
      argp ka = (argp)__builtin_amdgcn_kernarg_segment_ptr();
      unsigned bad, more, sto, s0, L, q0, h, u, n, ns, au, wp;
      asm volatile(
      "s_load_dwordx8 s[44:51], s[4:5], 0x0\n\t"
      "s_load_dwordx4 s[52:55], s[4:5], 0x20\n\t"
      "v_mbcnt_lo_u32_b32 v192, -1, 0\n\t"
      "v_mbcnt_hi_u32_b32 v192, -1, v192\n\t"
      "s_and_b32 s37, s6, 0x1f0\n\t"
      "s_cmp_lg_u32 s37, 0\n\t"
      "s_cbranch_scc1 L_npf_%=\n\t"
      "s_getpc_b64 s[56:57]\n\t"
      "L_pc_%=:\n\t"
      "s_lshl_b32 s37, s26, 13\n\t"
      "v_lshl_add_u32 v160, v192, 7, s37\n\t"
      "v_min_u32_e32 v160, L_end_%=-L_pc_%=, v160\n\t"
      "global_load_dword v160, v160, s[56:57]\n\t"
      "L_npf_%=:\n\t"
      "v_and_b32_e32 v158, 15, v192\n\t"
      "v_add_u32_e32 v144, s7, v158\n\t"
      "s_lshr_b32 s29, s6, 8\n\t"
      "s_bfe_u32 s37, s6, 0x40004\n\t"
      "s_xor_b32 s38, s37, 15\n\t"
      "s_bitcmp1_b32 s3, 0\n\t"
      "s_cselect_b32 s37, s38, s37\n\t"
      "s_lshl4_add_u32 s43, s3, s37\n\t"
      "s_lshl_b32 s37, s26, 15\n\t"
      "s_and_b32 s38, s26, 1\n\t"
      "s_lshl_b32 s38, s38, 5\n\t"
      "s_or_b32 s38, s38, s37\n\t"
      "v_bfe_u32 v193, v192, 2, 3\n\t"
      "v_lshlrev_b32_e32 v193, 12, v193\n\t"
      "v_lshrrev_b32_e32 v194, 5, v192\n\t"
      "v_lshl_add_u32 v193, v194, 6, v193\n\t"
      "v_and_b32_e32 v195, 3, v192\n\t"
      "v_lshl_add_u32 v224, v195, 4, v193\n\t"
      "s_waitcnt lgkmcnt(0)\n\t"
      "s_sub_u32 s30, s54, 1\n\t"
      "v_cmp_gt_u32_e64 s[56:57], s54, v144\n\t"
      "v_min_u32_e32 v144, s30, v144\n\t"
      "v_lshlrev_b32_e32 v144, 2, v144\n\t"
      "global_load_dwordx2 v[144:145], v144, s[50:51]\n\t"
      "s_lshl2_add_u32 s41, s29, s26\n\t"
      "s_xor_b32 s41, s41, 0\n\t"
      "s_cmp_ge_u32 s41, 2\n\t"
      "s_cbranch_scc1 L_nq_%=\n\t"
      "s_bfe_u32 s34, s6, 0x50003\n\t"
      "s_mul_i32 s34, s34, 2\n\t"
      "s_add_u32 s34, s34, s41\n\t"
      "v_lshrrev_b32_e32 v161, 2, v192\n\t"
      "v_lshl_add_u32 v161, s34, 4, v161\n\t"
      "v_mul_lo_u32 v161, v161, s55\n\t"
      "v_lshrrev_b32_e32 v161, 10, v161\n\t"
      "s_and_b32 s34, s6, 7\n\t"
      "s_lshl_b32 s34, s34, 9\n\t"
      "v_lshl_add_u32 v161, v161, 12, s34\n\t"
      "v_lshl_add_u32 v161, v195, 7, v161\n\t"
      "global_load_dword v161, v161, s[44:45]\n\t"
      "L_nq_%=:\n\t"
      "L_nq2_%=:\n\t"
      "v_xor_b32_e32 v251, s38, v224\n\t"
      "v_xor_b32_e32 v225, 0x8020, v224\n\t"
      "v_and_b32_e32 v196, 16, v192\n\t"
      "v_lshlrev_b32_e32 v195, 1, v196\n\t"
      "v_xor_b32_e32 v195, v195, v224\n\t"
      "v_add_u32_e32 v252, s37, v195\n\t"
      "v_sub_u32_e32 v154, 3, v194\n\t"
      "v_lshlrev_b32_e32 v159, 6, v154\n\t"
      "v_lshrrev_b32_e32 v155, 4, v196\n\t"
      "v_sub_u32_e32 v155, 3, v155\n\t"
      "v_lshl_or_b32 v156, v194, 4, v158\n\t"
      "v_xor_b32_e32 v156, 31, v156\n\t"
      "v_lshlrev_b32_e32 v156, 14, v156\n\t"
      "v_or_b32_e32 v157, v196, v158\n\t"
      "v_xor_b32_e32 v157, 31, v157\n\t"
      "v_lshlrev_b32_e32 v157, 14, v157\n\t"
      "s_mov_b32 s58, 0\n\t"
      "s_mov_b32 s59, -1\n\t"
      "s_mov_b32 s60, 0xffff0000\n\t"
      "s_mov_b32 s61, 0xffff0000\n\t"
      "s_mov_b32 s11, 0x20000\n\t"
      "s_mov_b32 s15, 0x20000\n\t"
      "s_mov_b32 s19, 0x20000\n\t"
      "s_mov_b32 s23, 0x20000\n\t"
      "s_lshl_b32 s42, s26, 11\n\t"
      "s_mov_b32 m0, s42\n\t"
      "s_mov_b32 s35, 16384\n\t"
      "s_mov_b32 s40, 32768\n\t"
      "s_mov_b32 s36, 0x3e0293ee\n\t"
      "s_mul_i32 s95, s29, 3\n\t"
      "s_add_u32 s34, s26, s29\n\t"
      "s_and_b32 s34, s34, 3\n\t"
      "s_xor_b32 s95, s95, s34\n\t"
      "s_bfe_u32 s93, s6, 0x10003\n\t"
      "s_xor_b32 s93, s93, s29\n\t"
      "s_and_b32 s25, s6, 7\n\t"
      "s_lshl1_add_u32 s93, s25, s93\n\t"
      "s_cmp_lt_u32 s41, 2\n\t"
      "s_cbranch_scc1 L_nq3_%=\n\t"
      "s_waitcnt vmcnt(0)\n\t"
      "L_nq3_%=:\n\t"
      "s_waitcnt vmcnt(1)\n\t"
      "v_sub_u32_e32 v145, v145, v144\n\t"
      "v_cndmask_b32_e64 v145, 0, v145, s[56:57]\n\t"
      "v_add_u32_e32 v148, 0xbf, v145\n\t"
      "v_lshrrev_b32_e32 v148, 6, v148\n\t"
      "v_add_u32_e32 v146, 31, v145\n\t"
      "v_add_u32_e32 v147, -2, v148\n\t"
      "v_mul_u32_u24_e32 v148, 0x5556, v148\n\t"
      "v_lshrrev_b32_e32 v146, 5, v146\n\t"
      "v_lshrrev_b32_e32 v148, 16, v148\n\t"
      "v_min_u32_e32 v146, 0x7ff, v146\n\t"
      "v_lshl_add_u32 v149, v148, 1, v148\n\t"
      "v_mul_u32_u24_e32 v151, v146, v154\n\t"
      "v_sub_u32_e32 v149, v149, v147\n\t"
      "v_mul_u32_u24_e32 v152, v146, v155\n\t"
      "v_min_u32_e32 v149, v149, v148\n\t"
      "v_lshl_or_b32 v151, v151, 19, v156\n\t"
      "v_sub_u32_e32 v150, v148, v149\n\t"
      "v_lshl_or_b32 v152, v152, 19, v157\n\t"
      "v_cndmask_b32_e64 v147, v150, v149, s[60:61]\n\t"
      "v_or_b32_e32 v152, v152, v147\n\t"
      "v_cndmask_b32_e64 v153, v150, v149, s[58:59]\n\t"
      "v_max_u32_e32 v164, v152, v151\n\t"
      "v_max_u32_dpp v165, v152, v151 row_ror:1 row_mask:0xf bank_mask:0xf\n\t"
      "v_max_u32_dpp v166, v152, v151 row_ror:2 row_mask:0xf bank_mask:0xf\n\t"
      "v_max_u32_dpp v167, v152, v151 row_ror:3 row_mask:0xf bank_mask:0xf\n\t"
      "v_max_u32_dpp v168, v152, v151 row_ror:4 row_mask:0xf bank_mask:0xf\n\t"
      "v_max_u32_dpp v169, v152, v151 row_ror:5 row_mask:0xf bank_mask:0xf\n\t"
      "v_max_u32_dpp v170, v152, v151 row_ror:6 row_mask:0xf bank_mask:0xf\n\t"
      "v_max_u32_dpp v171, v152, v151 row_ror:7 row_mask:0xf bank_mask:0xf\n\t"
      "v_max_u32_dpp v172, v152, v151 row_ror:8 row_mask:0xf bank_mask:0xf\n\t"
      "v_max_u32_dpp v173, v152, v151 row_ror:9 row_mask:0xf bank_mask:0xf\n\t"
      "v_max_u32_dpp v174, v152, v151 row_ror:10 row_mask:0xf bank_mask:0xf\n\t"
      "v_max_u32_dpp v175, v152, v151 row_ror:11 row_mask:0xf bank_mask:0xf\n\t"
      "v_max_u32_dpp v176, v152, v151 row_ror:12 row_mask:0xf bank_mask:0xf\n\t"
      "v_max_u32_dpp v177, v152, v151 row_ror:13 row_mask:0xf bank_mask:0xf\n\t"
      "v_max_u32_dpp v178, v152, v151 row_ror:14 row_mask:0xf bank_mask:0xf\n\t"
      "v_max_u32_dpp v179, v152, v151 row_ror:15 row_mask:0xf bank_mask:0xf\n\t"
      "v_add3_u32 v164, v164, v165, v166\n\t"
      "v_add3_u32 v167, v167, v168, v169\n\t"
      "v_add3_u32 v170, v170, v171, v172\n\t"
      "v_add3_u32 v173, v173, v174, v175\n\t"
      "v_add3_u32 v176, v176, v177, v178\n\t"
      "v_add3_u32 v164, v164, v167, v170\n\t"
      "v_add3_u32 v173, v173, v176, v179\n\t"
      "v_add_u32_e32 v164, v164, v173\n\t"
      "v_mov_b32_e32 v165, v164\n\t"
      "v_mul_u32_u24_e32 v150, 0xc0, v150\n\t"
      "v_add_u32_e32 v147, s43, v153\n\t"
      "v_permlane16_swap_b32_e32 v164, v165\n\t"
      "v_cndmask_b32_e64 v150, 0, v150, s[58:59]\n\t"
      "v_add_u32_e32 v164, v164, v165\n\t"
      "v_and_b32_e32 v164, 0x3fff, v164\n\t"
      "v_sub_u32_e32 v147, v147, v164\n\t"
      "v_cmp_lt_u32_e32 vcc, v147, v153\n\t"
      "v_mad_u32_u24 v147, v147, v159, v150\n\t"
      "s_mov_b32 s39, 2\n\t"
      "s_nop 0\n\t"
      "s_ff1_i32_b64 s37, vcc\n\t"
      "s_cbranch_vccz L_end_%=\n\t"
      "v_readlane_b32 s90, v144, s37\n\t"
      "v_readlane_b32 s91, v145, s37\n\t"
      "v_readlane_b32 s92, v147, s37\n\t"
      "s_lshl_b32 s38, s93, 8\n\t"
      "s_lshr_b32 s57, s90, 20\n\t"
      "s_lshl_b32 s56, s90, 12\n\t"
      "s_or_b32 s56, s56, s38\n\t"
      "s_add_u32 s12, s46, s56\n\t"
      "s_addc_u32 s13, s47, s57\n\t"
      "s_lshl_b32 s14, s91, 12\n\t"
      "s_sub_u32 s14, s14, 0xf00\n\t"
      "s_sub_u32 s30, s91, 1\n\t"
      "s_lshr_b32 s30, s30, 5\n\t"
      "s_lshl_b32 s31, s30, 17\n\t"
      "s_cmp_eq_u32 s29, 0\n\t"
      "s_cbranch_scc1 L_dl_%=\n\t"
      "s_sleep 24\n\t"
      "L_dl_%=:\n\t"
      "buffer_load_dwordx4 v251, s[12:15], s31 offen lds\n\t"
      "s_addk_i32 m0, 896\n\t"
      "s_mov_b32 s10, s14\n\t"
      "buffer_load_dwordx4 v251, s[12:15], s31 offen offset:128 lds\n\t"
      "s_add_u32 s8, s44, s56\n\t"
      "s_addc_u32 s9, s45, s57\n\t"
      "s_sub_u32 s38, s91, s92\n\t"
      "s_add_u32 s38, s38, 15\n\t"
      "s_lshr_b32 s38, s38, 4\n\t"
      "s_cmp_lt_u32 s37, 32\n\t"
      "s_cselect_b32 s37, 12, 8\n\t"
      "s_min_u32 s94, s38, s37\n\t"
      "s_max_u32 s37, s94, 8\n\t"
      "s_sub_u32 s37, s37, 8\n\t"
      "s_lshl_b32 s38, s95, 1\n\t"
      "s_cmp_lt_u32 s38, s94\n\t"
      "s_cselect_b32 s28, 2, 0\n\t"
      "s_cmp_lt_u32 s95, s37\n\t"
      "s_cselect_b32 s28, 3, s28\n\t"
      "s_min_u32 s37, s37, s95\n\t"
      "s_add_u32 s37, s37, s38\n\t"
      "s_lshl4_add_u32 s92, s37, s92\n\t"
      "s_lshl_b32 s27, s92, 12\n\t"
      "s_cmp_eq_u32 s28, 0\n\t"
      "s_cbranch_scc1 L_noq_%=\n\t"
      "s_cmp_eq_u32 s29, 0\n\t"
      "s_cbranch_scc1 L_th0_%=\n\t"
      "s_waitcnt vmcnt(0)\n\t"
      "L_th0_%=:\n\t"
      "s_add_u32 m0, s42, 32768\n\t"
      "s_add_u32 s37, s27, 0\n\t"
      "buffer_load_dwordx4 v224, s[8:11], s37 offen nt lds\n\t"
      "s_addk_i32 m0, 896\n\t"
      "s_nop 0\n\t"
      "buffer_load_dwordx4 v224, s[8:11], s37 offen offset:128 nt lds\n\t"
      "s_addk_i32 m0, 7296\n\t"
      "s_nop 0\n\t"
      "buffer_load_dwordx4 v225, s[8:11], s37 offen nt lds\n\t"
      "s_addk_i32 m0, 896\n\t"
      "s_nop 0\n\t"
      "buffer_load_dwordx4 v225, s[8:11], s37 offen offset:128 nt lds\n\t"
      "s_cmp_eq_u32 s29, 0\n\t"
      "s_cbranch_scc1 L_th1_%=\n\t"
      "s_waitcnt vmcnt(0)\n\t"
      "L_th1_%=:\n\t"
      "s_add_u32 m0, s42, 49152\n\t"
      "s_add_u32 s37, s27, 65536\n\t"
      "buffer_load_dwordx4 v224, s[8:11], s37 offen nt lds\n\t"
      "s_addk_i32 m0, 896\n\t"
      "s_nop 0\n\t"
      "buffer_load_dwordx4 v224, s[8:11], s37 offen offset:128 nt lds\n\t"
      "s_addk_i32 m0, 7296\n\t"
      "s_nop 0\n\t"
      "buffer_load_dwordx4 v225, s[8:11], s37 offen nt lds\n\t"
      "s_addk_i32 m0, 896\n\t"
      "s_nop 0\n\t"
      "buffer_load_dwordx4 v225, s[8:11], s37 offen offset:128 nt lds\n\t"
      "s_cmp_eq_u32 s28, 2\n\t"
      "s_cbranch_scc1 L_noq_%=\n\t"
      "s_cmp_eq_u32 s29, 0\n\t"
      "s_cbranch_scc1 L_noq_%=\n\t"
      "s_cmp_eq_u32 s29, 0\n\t"
      "s_cbranch_scc1 L_th2_%=\n\t"
      "s_waitcnt vmcnt(0)\n\t"
      "L_th2_%=:\n\t"
      "s_add_u32 m0, s42, 65536\n\t"
      "s_add_u32 s37, s27, 131072\n\t"
      "buffer_load_dwordx4 v224, s[8:11], s37 offen nt lds\n\t"
      "s_addk_i32 m0, 896\n\t"
      "s_nop 0\n\t"
      "buffer_load_dwordx4 v224, s[8:11], s37 offen offset:128 nt lds\n\t"
      "s_addk_i32 m0, 7296\n\t"
      "s_nop 0\n\t"
      "buffer_load_dwordx4 v225, s[8:11], s37 offen nt lds\n\t"
      "s_addk_i32 m0, 896\n\t"
      "s_nop 0\n\t"
      "buffer_load_dwordx4 v225, s[8:11], s37 offen offset:128 nt lds\n\t"
      "L_noq_%=:\n\t"
      "s_add_u32 s16, s48, s56\n\t"
      "s_addc_u32 s17, s49, s57\n\t"
      "s_mov_b32 s18, s10\n\t"
      "s_mov_b32 s41, s31\n\t"
      "s_mov_b32 s14, s31\n\t"
      "s_mov_b32 s31, 0\n\t"
      "s_cmp_eq_u32 s29, 0\n\t"
      "s_cselect_b32 s37, s28, 0\n\t"
      "s_cmp_lg_u32 s37, 0\n\t"
      "s_cbranch_scc1 L_np0_%=\n\t"
      "s_cmp_eq_u32 s29, 0\n\t"
      "s_cbranch_scc1 L_th3_%=\n\t"
      "s_waitcnt vmcnt(0)\n\t"
      "L_th3_%=:\n\t"
      "s_add_u32 m0, s42, 16384\n\t"
      "s_nop 0\n\t"
      "buffer_load_dwordx4 v251, s[12:15], s31 offen lds\n\t"
      "s_addk_i32 m0, 896\n\t"
      "s_nop 0\n\t"
      "buffer_load_dwordx4 v251, s[12:15], s31 offen offset:128 lds\n\t"
      "s_addk_i32 m0, 7296\n\t"
      "s_nop 0\n\t"
      "buffer_load_dwordx4 v252, s[16:19], s41 offen lds\n\t"
      "s_addk_i32 m0, 896\n\t"
      "s_nop 0\n\t"
      "buffer_load_dwordx4 v252, s[16:19], s41 offen offset:128 lds\n\t"
      "s_mov_b32 s18, s41\n\t"
      "s_mov_b32 s41, s31\n\t"
      "s_add_u32 s31, s31, 0x20000\n\t"
      "L_np0_%=:\n\t"
      "s_add_u32 s20, s52, s56\n\t"
      "s_addc_u32 s21, s53, s57\n\t"
      "s_mov_b32 s22, s10\n\t"
      "s_lshl_b32 s37, s30, 5\n\t"
      "s_sub_u32 s25, s91, s37\n\t"
      "s_add_u32 s30, s30, 1\n\t"
      "s_mov_b32 s24, s30\n\t"
      "s_mov_b32 s39, 0\n\t"
      "s_cmp_eq_u32 s29, 0\n\t"
      "s_cbranch_scc1 L_pr_%=\n\t"
      "s_setprio 1\n\t"
      "L_pr_%=:\n\t"
      "v_and_b32_e32 v193, 15, v192\n\t"
      "v_lshrrev_b32_e32 v194, 4, v192\n\t"
      "v_lshrrev_b32_e32 v195, 3, v193\n\t"
      "v_lshlrev_b32_e32 v196, 1, v195\n\t"
      "v_xor_b32_e32 v196, v196, v194\n\t"
      "v_and_b32_e32 v193, 7, v193\n\t"
      "v_lshlrev_b32_e32 v193, 6, v193\n\t"
      "v_lshl_add_u32 v193, v196, 4, v193\n\t"
      "v_lshl_add_u32 v248, v195, 11, v193\n\t"
      "v_lshl_add_u32 v254, v195, 13, v193\n\t"
      "s_add_u32 s34, s42, 32768\n\t"
      "v_add_u32_e32 v254, s34, v254\n\t"
      "v_lshrrev_b32_e32 v195, 1, v194\n\t"
      "v_lshlrev_b32_e32 v195, 11, v195\n\t"
      "v_and_b32_e32 v196, 1, v194\n\t"
      "v_mul_u32_u24_e32 v196, 288, v196\n\t"
      "v_bfe_u32 v193, v192, 2, 2\n\t"
      "v_lshl_add_u32 v195, v193, 6, v195\n\t"
      "v_and_b32_e32 v193, 3, v192\n\t"
      "v_lshl_add_u32 v195, v193, 3, v195\n\t"
      "s_movk_i32 s34, 0x2000\n\t"
      "v_add3_u32 v249, v195, v196, s34\n\t"
      "v_xor_b32_e32 v250, 32, v249\n\t"
      "v_lshlrev_b32_e32 v194, 2, v194\n\t"
      "v_mov_b32_e32 v196, 0xff800000\n\t"
      "v_mov_b64_e32 v[0:1], 0\n\t"
      "v_mov_b64_e32 v[2:3], 0\n\t"
      "v_mov_b64_e32 v[4:5], 0\n\t"
      "v_mov_b64_e32 v[6:7], 0\n\t"
      "v_mov_b64_e32 v[8:9], 0\n\t"
      "v_mov_b64_e32 v[10:11], 0\n\t"
      "v_mov_b64_e32 v[12:13], 0\n\t"
      "v_mov_b64_e32 v[14:15], 0\n\t"
      "v_mov_b64_e32 v[16:17], 0\n\t"
      "v_mov_b64_e32 v[18:19], 0\n\t"
      "v_mov_b64_e32 v[20:21], 0\n\t"
      "v_mov_b64_e32 v[22:23], 0\n\t"
      "v_mov_b64_e32 v[24:25], 0\n\t"
      "v_mov_b64_e32 v[26:27], 0\n\t"
      "v_mov_b64_e32 v[28:29], 0\n\t"
      "v_mov_b64_e32 v[30:31], 0\n\t"
      "v_mov_b64_e32 v[32:33], 0\n\t"
      "v_mov_b64_e32 v[34:35], 0\n\t"
      "v_mov_b64_e32 v[36:37], 0\n\t"
      "v_mov_b64_e32 v[38:39], 0\n\t"
      "v_mov_b64_e32 v[40:41], 0\n\t"
      "v_mov_b64_e32 v[42:43], 0\n\t"
      "v_mov_b64_e32 v[44:45], 0\n\t"
      "v_mov_b64_e32 v[46:47], 0\n\t"
      "v_mov_b64_e32 v[48:49], 0\n\t"
      "v_mov_b64_e32 v[50:51], 0\n\t"
      "v_mov_b64_e32 v[52:53], 0\n\t"
      "v_mov_b64_e32 v[54:55], 0\n\t"
      "v_mov_b64_e32 v[56:57], 0\n\t"
      "v_mov_b64_e32 v[58:59], 0\n\t"
      "v_mov_b64_e32 v[60:61], 0\n\t"
      "v_mov_b64_e32 v[62:63], 0\n\t"
      "v_mov_b64_e32 v[64:65], 0\n\t"
      "v_mov_b64_e32 v[66:67], 0\n\t"
      "v_mov_b64_e32 v[68:69], 0\n\t"
      "v_mov_b64_e32 v[70:71], 0\n\t"
      "v_mov_b64_e32 v[72:73], 0\n\t"
      "v_mov_b64_e32 v[74:75], 0\n\t"
      "v_mov_b64_e32 v[76:77], 0\n\t"
      "v_mov_b64_e32 v[78:79], 0\n\t"
      "v_mov_b64_e32 v[80:81], 0\n\t"
      "v_mov_b64_e32 v[82:83], 0\n\t"
      "v_mov_b64_e32 v[84:85], 0\n\t"
      "v_mov_b64_e32 v[86:87], 0\n\t"
      "v_mov_b64_e32 v[88:89], 0\n\t"
      "v_mov_b64_e32 v[90:91], 0\n\t"
      "v_mov_b64_e32 v[92:93], 0\n\t"
      "v_mov_b64_e32 v[94:95], 0\n\t"
      "v_mov_b64_e32 v[228:229], 0\n\t"
      "v_mov_b64_e32 v[230:231], 0\n\t"
      "v_mov_b64_e32 v[232:233], 0\n\t"
      "v_mov_b64_e32 v[234:235], 0\n\t"
      "v_mov_b64_e32 v[236:237], 0\n\t"
      "v_mov_b64_e32 v[238:239], 0\n\t"
      "v_cmp_eq_u32_e32 vcc, 0, v193\n\t"
      "v_mov_b32_e32 v240, 0x3f803f80\n\t"
      "s_nop 0\n\t"
      "v_cndmask_b32_e32 v240, 0, v240, vcc\n\t"
      "v_mov_b32_e32 v241, v240\n\t"
      "v_mov_b32_e32 v242, v240\n\t"
      "v_mov_b32_e32 v243, v240\n\t"
      "s_add_u32 s37, s43, 32\n\t"
      "v_cmp_lt_u32_e32 vcc, s37, v164\n\t"
      "v_sub_u32_e32 v195, s25, v194\n\t"
      "v_cmp_lt_i32_e64 s[44:45], 0, v195\n\t"
      "v_cmp_lt_i32_e64 s[46:47], 1, v195\n\t"
      "v_cmp_lt_i32_e64 s[48:49], 2, v195\n\t"
      "v_cmp_lt_i32_e64 s[50:51], 3, v195\n\t"
      "v_cndmask_b32_e64 v176, v196, 0, s[44:45]\n\t"
      "v_cndmask_b32_e64 v177, v196, 0, s[46:47]\n\t"
      "v_cndmask_b32_e64 v178, v196, 0, s[48:49]\n\t"
      "v_cndmask_b32_e64 v179, v196, 0, s[50:51]\n\t"
      "v_cmp_lt_i32_e64 s[44:45], 16, v195\n\t"
      "v_cmp_lt_i32_e64 s[46:47], 17, v195\n\t"
      "v_cmp_lt_i32_e64 s[48:49], 18, v195\n\t"
      "v_cmp_lt_i32_e64 s[50:51], 19, v195\n\t"
      "v_cndmask_b32_e64 v180, v196, 0, s[44:45]\n\t"
      "v_cndmask_b32_e64 v181, v196, 0, s[46:47]\n\t"
      "v_cndmask_b32_e64 v182, v196, 0, s[48:49]\n\t"
      "v_cndmask_b32_e64 v183, v196, 0, s[50:51]\n\t"
      "s_or_b32 s43, vcc_lo, vcc_hi\n\t"
      "s_cmp_lg_u32 s29, 0\n\t"
      "s_cselect_b32 s59, 4, 3\n\t"
      "s_cselect_b64 vcc, -1, 0\n\t"
      "s_sub_u32 s40, s59, 1\n\t"
      "s_lshl_b32 s40, s40, 14\n\t"
      "s_sub_u32 s58, 0, s40\n\t"
      "s_cmp_eq_u32 s28, 0\n\t"
      "s_cbranch_scc1 L_idle_%=\n\t"
      "s_cmp_eq_u32 s28, 2\n\t"
      "s_cbranch_vccnz L_B_%=\n\t"
      "s_cbranch_scc1 L_A2_%=\n\t"
      "L_A3_%=:\n\t"
      "s_waitcnt vmcnt(8)\n\t"
      "s_barrier\n\t"
      "ds_read_b128 v[160:163], v248 offset:0\n\t"
      "ds_read_b128 v[164:167], v248 offset:512\n\t"
      "ds_read_b128 v[168:171], v248 offset:1024\n\t"
      "ds_read_b128 v[172:175], v248 offset:1536\n\t"
      "ds_read_b128 v[144:147], v248 offset:4096\n\t"
      "ds_read_b128 v[148:151], v248 offset:4608\n\t"
      "ds_read_b128 v[152:155], v248 offset:5120\n\t"
      "ds_read_b128 v[156:159], v248 offset:5632\n\t"
      "s_add_u32 m0, s42, 65536\n\t"
      "s_add_u32 s37, s27, 131072\n\t"
      "buffer_load_dwordx4 v224, s[8:11], s37 offen nt lds\n\t"
      "s_addk_i32 m0, 896\n\t"
      "s_nop 0\n\t"
      "buffer_load_dwordx4 v224, s[8:11], s37 offen offset:128 nt lds\n\t"
      "s_addk_i32 m0, 7296\n\t"
      "s_nop 0\n\t"
      "buffer_load_dwordx4 v225, s[8:11], s37 offen nt lds\n\t"
      "s_addk_i32 m0, 896\n\t"
      "s_nop 0\n\t"
      "buffer_load_dwordx4 v225, s[8:11], s37 offen offset:128 nt lds\n\t"
      "s_waitcnt vmcnt(8)\n\t"
      "ds_read_b128 v[96:99], v254 offset:0\n\t"
      "ds_read_b128 v[100:103], v254 offset:512\n\t"
      "ds_read_b128 v[104:107], v254 offset:1024\n\t"
      "ds_read_b128 v[108:111], v254 offset:1536\n\t"
      "s_add_u32 m0, s42, 16384\n\t"
      "s_nop 0\n\t"
      "buffer_load_dwordx4 v251, s[12:15], s31 offen lds\n\t"
      "s_addk_i32 m0, 896\n\t"
      "s_nop 0\n\t"
      "buffer_load_dwordx4 v251, s[12:15], s31 offen offset:128 lds\n\t"
      "s_addk_i32 m0, 7296\n\t"
      "s_nop 0\n\t"
      "buffer_load_dwordx4 v252, s[16:19], s41 offen lds\n\t"
      "s_addk_i32 m0, 896\n\t"
      "s_nop 0\n\t"
      "buffer_load_dwordx4 v252, s[16:19], s41 offen offset:128 lds\n\t"
      "s_mov_b32 s18, s41\n\t"
      "s_mov_b32 s41, s31\n\t"
      "s_add_u32 s31, s31, 0x20000\n\t"
      "s_waitcnt lgkmcnt(3)\n\t"
      "v_lshlrev_b32_e32 v192, 16, v96\n\t"
      "v_and_b32_e32 v193, s60, v96\n\t"
      "v_lshlrev_b32_e32 v194, 16, v97\n\t"
      "v_and_b32_e32 v195, s60, v97\n\t"
      "v_lshlrev_b32_e32 v196, 16, v98\n\t"
      "v_and_b32_e32 v197, s60, v98\n\t"
      "v_lshlrev_b32_e32 v198, 16, v99\n\t"
      "v_and_b32_e32 v199, s60, v99\n\t"
      "v_pk_mul_f32 v[192:193], v[192:193], s[36:37] op_sel_hi:[1,0]\n\t"
      "v_pk_mul_f32 v[194:195], v[194:195], s[36:37] op_sel_hi:[1,0]\n\t"
      "v_pk_mul_f32 v[196:197], v[196:197], s[36:37] op_sel_hi:[1,0]\n\t"
      "v_pk_mul_f32 v[198:199], v[198:199], s[36:37] op_sel_hi:[1,0]\n\t"
      "v_cvt_pk_bf16_f32 v96, v192, v193\n\t"
      "v_cvt_pk_bf16_f32 v97, v194, v195\n\t"
      "v_cvt_pk_bf16_f32 v98, v196, v197\n\t"
      "v_cvt_pk_bf16_f32 v99, v198, v199\n\t"
      "s_waitcnt lgkmcnt(2)\n\t"
      "v_lshlrev_b32_e32 v192, 16, v100\n\t"
      "v_and_b32_e32 v193, s60, v100\n\t"
      "v_lshlrev_b32_e32 v194, 16, v101\n\t"
      "v_and_b32_e32 v195, s60, v101\n\t"
      "v_lshlrev_b32_e32 v196, 16, v102\n\t"
      "v_and_b32_e32 v197, s60, v102\n\t"
      "v_lshlrev_b32_e32 v198, 16, v103\n\t"
      "v_and_b32_e32 v199, s60, v103\n\t"
      "v_pk_mul_f32 v[192:193], v[192:193], s[36:37] op_sel_hi:[1,0]\n\t"
      "v_pk_mul_f32 v[194:195], v[194:195], s[36:37] op_sel_hi:[1,0]\n\t"
      "v_pk_mul_f32 v[196:197], v[196:197], s[36:37] op_sel_hi:[1,0]\n\t"
      "v_pk_mul_f32 v[198:199], v[198:199], s[36:37] op_sel_hi:[1,0]\n\t"
      "v_cvt_pk_bf16_f32 v100, v192, v193\n\t"
      "v_cvt_pk_bf16_f32 v101, v194, v195\n\t"
      "v_cvt_pk_bf16_f32 v102, v196, v197\n\t"
      "v_cvt_pk_bf16_f32 v103, v198, v199\n\t"
      "s_waitcnt lgkmcnt(1)\n\t"
      "v_lshlrev_b32_e32 v192, 16, v104\n\t"
      "v_and_b32_e32 v193, s60, v104\n\t"
      "v_lshlrev_b32_e32 v194, 16, v105\n\t"
      "v_and_b32_e32 v195, s60, v105\n\t"
      "v_lshlrev_b32_e32 v196, 16, v106\n\t"
      "v_and_b32_e32 v197, s60, v106\n\t"
      "v_lshlrev_b32_e32 v198, 16, v107\n\t"
      "v_and_b32_e32 v199, s60, v107\n\t"
      "v_pk_mul_f32 v[192:193], v[192:193], s[36:37] op_sel_hi:[1,0]\n\t"
      "v_pk_mul_f32 v[194:195], v[194:195], s[36:37] op_sel_hi:[1,0]\n\t"
      "v_pk_mul_f32 v[196:197], v[196:197], s[36:37] op_sel_hi:[1,0]\n\t"
      "v_pk_mul_f32 v[198:199], v[198:199], s[36:37] op_sel_hi:[1,0]\n\t"
      "v_cvt_pk_bf16_f32 v104, v192, v193\n\t"
      "v_cvt_pk_bf16_f32 v105, v194, v195\n\t"
      "v_cvt_pk_bf16_f32 v106, v196, v197\n\t"
      "v_cvt_pk_bf16_f32 v107, v198, v199\n\t"
      "s_waitcnt lgkmcnt(0)\n\t"
      "v_lshlrev_b32_e32 v192, 16, v108\n\t"
      "v_and_b32_e32 v193, s60, v108\n\t"
      "v_lshlrev_b32_e32 v194, 16, v109\n\t"
      "v_and_b32_e32 v195, s60, v109\n\t"
      "v_lshlrev_b32_e32 v196, 16, v110\n\t"
      "v_and_b32_e32 v197, s60, v110\n\t"
      "v_lshlrev_b32_e32 v198, 16, v111\n\t"
      "v_and_b32_e32 v199, s60, v111\n\t"
      "v_pk_mul_f32 v[192:193], v[192:193], s[36:37] op_sel_hi:[1,0]\n\t"
      "v_pk_mul_f32 v[194:195], v[194:195], s[36:37] op_sel_hi:[1,0]\n\t"
      "v_pk_mul_f32 v[196:197], v[196:197], s[36:37] op_sel_hi:[1,0]\n\t"
      "v_pk_mul_f32 v[198:199], v[198:199], s[36:37] op_sel_hi:[1,0]\n\t"
      "v_cvt_pk_bf16_f32 v108, v192, v193\n\t"
      "v_cvt_pk_bf16_f32 v109, v194, v195\n\t"
      "v_cvt_pk_bf16_f32 v110, v196, v197\n\t"
      "v_cvt_pk_bf16_f32 v111, v198, v199\n\t"
      "v_mfma_f32_16x16x32_bf16 v[192:195], v[160:163], v[96:99], v[176:179]\n\t"
      "v_mfma_f32_16x16x32_bf16 v[192:195], v[164:167], v[100:103], v[192:195]\n\t"
      "v_mfma_f32_16x16x32_bf16 v[192:195], v[168:171], v[104:107], v[192:195]\n\t"
      "v_mfma_f32_16x16x32_bf16 v[192:195], v[172:175], v[108:111], v[192:195]\n\t"
      "v_mfma_f32_16x16x32_bf16 v[196:199], v[144:147], v[96:99], v[180:183]\n\t"
      "v_mfma_f32_16x16x32_bf16 v[196:199], v[148:151], v[100:103], v[196:199]\n\t"
      "v_mfma_f32_16x16x32_bf16 v[196:199], v[152:155], v[104:107], v[196:199]\n\t"
      "v_mfma_f32_16x16x32_bf16 v[196:199], v[156:159], v[108:111], v[196:199]\n\t"
      "s_nop 3\n\t"
      "v_max3_f32 v184, v192, v193, v194\n\t"
      "s_nop 2\n\t"
      "v_max3_f32 v253, v195, v196, v197\n\t"
      "v_max3_f32 v184, v184, v198, v199\n\t"
      "v_max_f32_e32 v184, v184, v253\n\t"
      "v_mov_b32_e32 v253, v184\n\t"
      "s_nop 1\n\t"
      "v_permlane16_swap_b32_e32 v184, v253\n\t"
      "s_nop 0\n\t"
      "v_max_f32_e32 v184, v184, v253\n\t"
      "v_mov_b32_e32 v253, v184\n\t"
      "s_nop 1\n\t"
      "v_permlane32_swap_b32_e32 v184, v253\n\t"
      "s_nop 0\n\t"
      "v_max_f32_e32 v184, v184, v253\n\t"
      "v_sub_f32_e32 v192, v192, v184\n\t"
      "v_sub_f32_e32 v193, v193, v184\n\t"
      "v_sub_f32_e32 v194, v194, v184\n\t"
      "v_sub_f32_e32 v195, v195, v184\n\t"
      "v_sub_f32_e32 v196, v196, v184\n\t"
      "v_sub_f32_e32 v197, v197, v184\n\t"
      "v_sub_f32_e32 v198, v198, v184\n\t"
      "v_sub_f32_e32 v199, v199, v184\n\t"
      "v_sub_f32_e32 v187, 0, v184\n\t"
      "v_sub_f32_e32 v186, 0, v184\n\t"
      "v_sub_f32_e32 v185, 0, v184\n\t"
      "v_sub_f32_e32 v184, 0, v184\n\t"
      "v_exp_f32_e32 v194, v194\n\t"
      "v_exp_f32_e32 v192, v192\n\t"
      "v_exp_f32_e32 v193, v193\n\t"
      "v_exp_f32_e32 v195, v195\n\t"
      "v_exp_f32_e32 v196, v196\n\t"
      "v_exp_f32_e32 v198, v198\n\t"
      "v_exp_f32_e32 v197, v197\n\t"
      "v_exp_f32_e32 v199, v199\n\t"
      "v_cvt_pk_bf16_f32 v217, v194, v195\n\t"
      "v_cvt_pk_bf16_f32 v216, v192, v193\n\t"
      "v_cvt_pk_bf16_f32 v219, v198, v199\n\t"
      "v_cvt_pk_bf16_f32 v218, v196, v197\n\t"
      "s_waitcnt vmcnt(8)\n\t"
      "ds_read_b128 v[112:115], v254 offset:16384\n\t"
      "ds_read_b128 v[116:119], v254 offset:16896\n\t"
      "ds_read_b128 v[120:123], v254 offset:17408\n\t"
      "ds_read_b128 v[124:127], v254 offset:17920\n\t"
      "s_add_u32 m0, s42, 32768\n\t"
      "s_nop 0\n\t"
      "buffer_load_dwordx4 v251, s[12:15], s31 offen lds\n\t"
      "s_addk_i32 m0, 896\n\t"
      "s_nop 0\n\t"
      "buffer_load_dwordx4 v251, s[12:15], s31 offen offset:128 lds\n\t"
      "s_addk_i32 m0, 7296\n\t"
      "s_nop 0\n\t"
      "buffer_load_dwordx4 v252, s[16:19], s41 offen lds\n\t"
      "s_addk_i32 m0, 896\n\t"
      "s_nop 0\n\t"
      "buffer_load_dwordx4 v252, s[16:19], s41 offen offset:128 lds\n\t"
      "s_mov_b32 s41, s31\n\t"
      "s_add_u32 s31, s31, 0x20000\n\t"
      "s_waitcnt lgkmcnt(3)\n\t"
      "v_lshlrev_b32_e32 v200, 16, v112\n\t"
      "v_and_b32_e32 v201, s60, v112\n\t"
      "v_lshlrev_b32_e32 v202, 16, v113\n\t"
      "v_and_b32_e32 v203, s60, v113\n\t"
      "v_lshlrev_b32_e32 v204, 16, v114\n\t"
      "v_and_b32_e32 v205, s60, v114\n\t"
      "v_lshlrev_b32_e32 v206, 16, v115\n\t"
      "v_and_b32_e32 v207, s60, v115\n\t"
      "v_pk_mul_f32 v[200:201], v[200:201], s[36:37] op_sel_hi:[1,0]\n\t"
      "v_pk_mul_f32 v[202:203], v[202:203], s[36:37] op_sel_hi:[1,0]\n\t"
      "v_pk_mul_f32 v[204:205], v[204:205], s[36:37] op_sel_hi:[1,0]\n\t"
      "v_pk_mul_f32 v[206:207], v[206:207], s[36:37] op_sel_hi:[1,0]\n\t"
      "v_cvt_pk_bf16_f32 v112, v200, v201\n\t"
      "v_cvt_pk_bf16_f32 v113, v202, v203\n\t"
      "v_cvt_pk_bf16_f32 v114, v204, v205\n\t"
      "v_cvt_pk_bf16_f32 v115, v206, v207\n\t"
      "s_waitcnt lgkmcnt(2)\n\t"
      "v_lshlrev_b32_e32 v200, 16, v116\n\t"
      "v_and_b32_e32 v201, s60, v116\n\t"
      "v_lshlrev_b32_e32 v202, 16, v117\n\t"
      "v_and_b32_e32 v203, s60, v117\n\t"
      "v_lshlrev_b32_e32 v204, 16, v118\n\t"
      "v_and_b32_e32 v205, s60, v118\n\t"
      "v_lshlrev_b32_e32 v206, 16, v119\n\t"
      "v_and_b32_e32 v207, s60, v119\n\t"
      "v_pk_mul_f32 v[200:201], v[200:201], s[36:37] op_sel_hi:[1,0]\n\t"
      "v_pk_mul_f32 v[202:203], v[202:203], s[36:37] op_sel_hi:[1,0]\n\t"
      "v_pk_mul_f32 v[204:205], v[204:205], s[36:37] op_sel_hi:[1,0]\n\t"
      "v_pk_mul_f32 v[206:207], v[206:207], s[36:37] op_sel_hi:[1,0]\n\t"
      "v_cvt_pk_bf16_f32 v116, v200, v201\n\t"
      "v_cvt_pk_bf16_f32 v117, v202, v203\n\t"
      "v_cvt_pk_bf16_f32 v118, v204, v205\n\t"
      "v_cvt_pk_bf16_f32 v119, v206, v207\n\t"
      "s_waitcnt lgkmcnt(1)\n\t"
      "v_lshlrev_b32_e32 v200, 16, v120\n\t"
      "v_and_b32_e32 v201, s60, v120\n\t"
      "v_lshlrev_b32_e32 v202, 16, v121\n\t"
      "v_and_b32_e32 v203, s60, v121\n\t"
      "v_lshlrev_b32_e32 v204, 16, v122\n\t"
      "v_and_b32_e32 v205, s60, v122\n\t"
      "v_lshlrev_b32_e32 v206, 16, v123\n\t"
      "v_and_b32_e32 v207, s60, v123\n\t"
      "v_pk_mul_f32 v[200:201], v[200:201], s[36:37] op_sel_hi:[1,0]\n\t"
      "v_pk_mul_f32 v[202:203], v[202:203], s[36:37] op_sel_hi:[1,0]\n\t"
      "v_pk_mul_f32 v[204:205], v[204:205], s[36:37] op_sel_hi:[1,0]\n\t"
      "v_pk_mul_f32 v[206:207], v[206:207], s[36:37] op_sel_hi:[1,0]\n\t"
      "v_cvt_pk_bf16_f32 v120, v200, v201\n\t"
      "v_cvt_pk_bf16_f32 v121, v202, v203\n\t"
      "v_cvt_pk_bf16_f32 v122, v204, v205\n\t"
      "v_cvt_pk_bf16_f32 v123, v206, v207\n\t"
      "s_waitcnt lgkmcnt(0)\n\t"
      "v_lshlrev_b32_e32 v200, 16, v124\n\t"
      "v_and_b32_e32 v201, s60, v124\n\t"
      "v_lshlrev_b32_e32 v202, 16, v125\n\t"
      "v_and_b32_e32 v203, s60, v125\n\t"
      "v_lshlrev_b32_e32 v204, 16, v126\n\t"
      "v_and_b32_e32 v205, s60, v126\n\t"
      "v_lshlrev_b32_e32 v206, 16, v127\n\t"
      "v_and_b32_e32 v207, s60, v127\n\t"
      "v_pk_mul_f32 v[200:201], v[200:201], s[36:37] op_sel_hi:[1,0]\n\t"
      "v_pk_mul_f32 v[202:203], v[202:203], s[36:37] op_sel_hi:[1,0]\n\t"
      "v_pk_mul_f32 v[204:205], v[204:205], s[36:37] op_sel_hi:[1,0]\n\t"
      "v_pk_mul_f32 v[206:207], v[206:207], s[36:37] op_sel_hi:[1,0]\n\t"
      "v_cvt_pk_bf16_f32 v124, v200, v201\n\t"
      "v_cvt_pk_bf16_f32 v125, v202, v203\n\t"
      "v_cvt_pk_bf16_f32 v126, v204, v205\n\t"
      "v_cvt_pk_bf16_f32 v127, v206, v207\n\t"
      "v_mfma_f32_16x16x32_bf16 v[200:203], v[160:163], v[112:115], v[176:179]\n\t"
      "v_mfma_f32_16x16x32_bf16 v[200:203], v[164:167], v[116:119], v[200:203]\n\t"
      "v_mfma_f32_16x16x32_bf16 v[200:203], v[168:171], v[120:123], v[200:203]\n\t"
      "v_mfma_f32_16x16x32_bf16 v[200:203], v[172:175], v[124:127], v[200:203]\n\t"
      "v_mfma_f32_16x16x32_bf16 v[204:207], v[144:147], v[112:115], v[180:183]\n\t"
      "v_mfma_f32_16x16x32_bf16 v[204:207], v[148:151], v[116:119], v[204:207]\n\t"
      "v_mfma_f32_16x16x32_bf16 v[204:207], v[152:155], v[120:123], v[204:207]\n\t"
      "v_mfma_f32_16x16x32_bf16 v[204:207], v[156:159], v[124:127], v[204:207]\n\t"
      "s_nop 3\n\t"
      "v_max3_f32 v188, v200, v201, v202\n\t"
      "s_nop 2\n\t"
      "v_max3_f32 v253, v203, v204, v205\n\t"
      "v_max3_f32 v188, v188, v206, v207\n\t"
      "v_max_f32_e32 v188, v188, v253\n\t"
      "v_mov_b32_e32 v253, v188\n\t"
      "s_nop 1\n\t"
      "v_permlane16_swap_b32_e32 v188, v253\n\t"
      "s_nop 0\n\t"
      "v_max_f32_e32 v188, v188, v253\n\t"
      "v_mov_b32_e32 v253, v188\n\t"
      "s_nop 1\n\t"
      "v_permlane32_swap_b32_e32 v188, v253\n\t"
      "s_nop 0\n\t"
      "v_max_f32_e32 v188, v188, v253\n\t"
      "v_sub_f32_e32 v200, v200, v188\n\t"
      "v_sub_f32_e32 v201, v201, v188\n\t"
      "v_sub_f32_e32 v202, v202, v188\n\t"
      "v_sub_f32_e32 v203, v203, v188\n\t"
      "v_sub_f32_e32 v204, v204, v188\n\t"
      "v_sub_f32_e32 v205, v205, v188\n\t"
      "v_sub_f32_e32 v206, v206, v188\n\t"
      "v_sub_f32_e32 v207, v207, v188\n\t"
      "v_sub_f32_e32 v191, 0, v188\n\t"
      "v_sub_f32_e32 v190, 0, v188\n\t"
      "v_sub_f32_e32 v189, 0, v188\n\t"
      "v_sub_f32_e32 v188, 0, v188\n\t"
      "v_exp_f32_e32 v202, v202\n\t"
      "v_exp_f32_e32 v203, v203\n\t"
      "v_exp_f32_e32 v200, v200\n\t"
      "v_exp_f32_e32 v201, v201\n\t"
      "v_exp_f32_e32 v206, v206\n\t"
      "v_exp_f32_e32 v207, v207\n\t"
      "v_exp_f32_e32 v204, v204\n\t"
      "v_exp_f32_e32 v205, v205\n\t"
      "v_cvt_pk_bf16_f32 v221, v202, v203\n\t"
      "v_cvt_pk_bf16_f32 v220, v200, v201\n\t"
      "v_cvt_pk_bf16_f32 v223, v206, v207\n\t"
      "v_cvt_pk_bf16_f32 v222, v204, v205\n\t"
      "s_waitcnt vmcnt(8)\n\t"
      "ds_read_b128 v[128:131], v254 offset:32768\n\t"
      "ds_read_b128 v[132:135], v254 offset:33280\n\t"
      "ds_read_b128 v[136:139], v254 offset:33792\n\t"
      "ds_read_b128 v[140:143], v254 offset:34304\n\t"
      "s_waitcnt lgkmcnt(3)\n\t"
      "v_lshlrev_b32_e32 v208, 16, v128\n\t"
      "v_and_b32_e32 v209, s60, v128\n\t"
      "v_lshlrev_b32_e32 v210, 16, v129\n\t"
      "v_and_b32_e32 v211, s60, v129\n\t"
      "v_lshlrev_b32_e32 v212, 16, v130\n\t"
      "v_and_b32_e32 v213, s60, v130\n\t"
      "v_lshlrev_b32_e32 v214, 16, v131\n\t"
      "v_and_b32_e32 v215, s60, v131\n\t"
      "v_pk_mul_f32 v[208:209], v[208:209], s[36:37] op_sel_hi:[1,0]\n\t"
      "v_pk_mul_f32 v[210:211], v[210:211], s[36:37] op_sel_hi:[1,0]\n\t"
      "v_pk_mul_f32 v[212:213], v[212:213], s[36:37] op_sel_hi:[1,0]\n\t"
      "v_pk_mul_f32 v[214:215], v[214:215], s[36:37] op_sel_hi:[1,0]\n\t"
      "v_cvt_pk_bf16_f32 v128, v208, v209\n\t"
      "v_cvt_pk_bf16_f32 v129, v210, v211\n\t"
      "v_cvt_pk_bf16_f32 v130, v212, v213\n\t"
      "v_cvt_pk_bf16_f32 v131, v214, v215\n\t"
      "s_waitcnt lgkmcnt(2)\n\t"
      "v_lshlrev_b32_e32 v208, 16, v132\n\t"
      "v_and_b32_e32 v209, s60, v132\n\t"
      "v_lshlrev_b32_e32 v210, 16, v133\n\t"
      "v_and_b32_e32 v211, s60, v133\n\t"
      "v_lshlrev_b32_e32 v212, 16, v134\n\t"
      "v_and_b32_e32 v213, s60, v134\n\t"
      "v_lshlrev_b32_e32 v214, 16, v135\n\t"
      "v_and_b32_e32 v215, s60, v135\n\t"
      "v_pk_mul_f32 v[208:209], v[208:209], s[36:37] op_sel_hi:[1,0]\n\t"
      "v_pk_mul_f32 v[210:211], v[210:211], s[36:37] op_sel_hi:[1,0]\n\t"
      "v_pk_mul_f32 v[212:213], v[212:213], s[36:37] op_sel_hi:[1,0]\n\t"
      "v_pk_mul_f32 v[214:215], v[214:215], s[36:37] op_sel_hi:[1,0]\n\t"
      "v_cvt_pk_bf16_f32 v132, v208, v209\n\t"
      "v_cvt_pk_bf16_f32 v133, v210, v211\n\t"
      "v_cvt_pk_bf16_f32 v134, v212, v213\n\t"
      "v_cvt_pk_bf16_f32 v135, v214, v215\n\t"
      "s_waitcnt lgkmcnt(1)\n\t"
      "v_lshlrev_b32_e32 v208, 16, v136\n\t"
      "v_and_b32_e32 v209, s60, v136\n\t"
      "v_lshlrev_b32_e32 v210, 16, v137\n\t"
      "v_and_b32_e32 v211, s60, v137\n\t"
      "v_lshlrev_b32_e32 v212, 16, v138\n\t"
      "v_and_b32_e32 v213, s60, v138\n\t"
      "v_lshlrev_b32_e32 v214, 16, v139\n\t"
      "v_and_b32_e32 v215, s60, v139\n\t"
      "v_pk_mul_f32 v[208:209], v[208:209], s[36:37] op_sel_hi:[1,0]\n\t"
      "v_pk_mul_f32 v[210:211], v[210:211], s[36:37] op_sel_hi:[1,0]\n\t"
      "v_pk_mul_f32 v[212:213], v[212:213], s[36:37] op_sel_hi:[1,0]\n\t"
      "v_pk_mul_f32 v[214:215], v[214:215], s[36:37] op_sel_hi:[1,0]\n\t"
      "v_cvt_pk_bf16_f32 v136, v208, v209\n\t"
      "v_cvt_pk_bf16_f32 v137, v210, v211\n\t"
      "v_cvt_pk_bf16_f32 v138, v212, v213\n\t"
      "v_cvt_pk_bf16_f32 v139, v214, v215\n\t"
      "s_waitcnt lgkmcnt(0)\n\t"
      "v_lshlrev_b32_e32 v208, 16, v140\n\t"
      "v_and_b32_e32 v209, s60, v140\n\t"
      "v_lshlrev_b32_e32 v210, 16, v141\n\t"
      "v_and_b32_e32 v211, s60, v141\n\t"
      "v_lshlrev_b32_e32 v212, 16, v142\n\t"
      "v_and_b32_e32 v213, s60, v142\n\t"
      "v_lshlrev_b32_e32 v214, 16, v143\n\t"
      "v_and_b32_e32 v215, s60, v143\n\t"
      "v_pk_mul_f32 v[208:209], v[208:209], s[36:37] op_sel_hi:[1,0]\n\t"
      "v_pk_mul_f32 v[210:211], v[210:211], s[36:37] op_sel_hi:[1,0]\n\t"
      "v_pk_mul_f32 v[212:213], v[212:213], s[36:37] op_sel_hi:[1,0]\n\t"
      "v_pk_mul_f32 v[214:215], v[214:215], s[36:37] op_sel_hi:[1,0]\n\t"
      "v_cvt_pk_bf16_f32 v140, v208, v209\n\t"
      "v_cvt_pk_bf16_f32 v141, v210, v211\n\t"
      "v_cvt_pk_bf16_f32 v142, v212, v213\n\t"
      "v_cvt_pk_bf16_f32 v143, v214, v215\n\t"
      "v_mfma_f32_16x16x32_bf16 v[208:211], v[160:163], v[128:131], v[176:179]\n\t"
      "v_mfma_f32_16x16x32_bf16 v[208:211], v[164:167], v[132:135], v[208:211]\n\t"
      "v_mfma_f32_16x16x32_bf16 v[208:211], v[168:171], v[136:139], v[208:211]\n\t"
      "v_mfma_f32_16x16x32_bf16 v[208:211], v[172:175], v[140:143], v[208:211]\n\t"
      "v_mfma_f32_16x16x32_bf16 v[212:215], v[144:147], v[128:131], v[180:183]\n\t"
      "v_mfma_f32_16x16x32_bf16 v[212:215], v[148:151], v[132:135], v[212:215]\n\t"
      "v_mfma_f32_16x16x32_bf16 v[212:215], v[152:155], v[136:139], v[212:215]\n\t"
      "v_mfma_f32_16x16x32_bf16 v[212:215], v[156:159], v[140:143], v[212:215]\n\t"
      "s_cmp_lt_u32 s42, s40\n\t"
      "s_cselect_b32 s34, s35, s58\n\t"
      "s_mov_b32 m0, s42\n\t"
      "s_add_u32 s42, s42, s34\n\t"
      "v_add_u32_e32 v248, s34, v248\n\t"
      "v_add_u32_e32 v249, s34, v249\n\t"
      "v_add_u32_e32 v250, s34, v250\n\t"
      "s_cmp_lg_u32 s30, 1\n\t"
      "s_cbranch_scc1 L_pw_A3_%=_0\n\t"
      "s_waitcnt vmcnt(0)\n\t"
      "L_pw_A3_%=_0:\n\t"
      "s_waitcnt vmcnt(4)\n\t"
      "s_barrier\n\t"
      "ds_read_b128 v[168:171], v248 offset:1024\n\t"
      "ds_read_b128 v[172:175], v248 offset:1536\n\t"
      "ds_read_b128 v[160:163], v248 offset:0\n\t"
      "ds_read_b128 v[164:167], v248 offset:512\n\t"
      "ds_read_b128 v[144:147], v248 offset:4096\n\t"
      "ds_read_b128 v[148:151], v248 offset:4608\n\t"
      "ds_read_b128 v[152:155], v248 offset:5120\n\t"
      "ds_read_b128 v[156:159], v248 offset:5632\n\t"
      "s_cmp_lt_u32 s30, 3\n\t"
      "s_cbranch_scc1 L_pnd_A3_%=\n\t"
      "s_nop 0\n\t"
      "buffer_load_dwordx4 v251, s[12:15], s31 offen lds\n\t"
      "s_addk_i32 m0, 896\n\t"
      "s_nop 0\n\t"
      "buffer_load_dwordx4 v251, s[12:15], s31 offen offset:128 lds\n\t"
      "s_addk_i32 m0, 7296\n\t"
      "s_nop 0\n\t"
      "buffer_load_dwordx4 v252, s[16:19], s41 offen lds\n\t"
      "s_addk_i32 m0, 896\n\t"
      "s_nop 0\n\t"
      "buffer_load_dwordx4 v252, s[16:19], s41 offen offset:128 lds\n\t"
      "s_mov_b32 s41, s31\n\t"
      "s_add_u32 s31, s31, 0x20000\n\t"
      "L_pnd_A3_%=:\n\t"
      "s_sub_i32 s25, s30, 4\n\t"
      "s_max_i32 s25, s25, 3\n\t"
      "v_max3_f32 v244, v208, v209, v210\n\t"
      "v_max3_f32 v253, v211, v212, v213\n\t"
      "v_max3_f32 v244, v244, v214, v215\n\t"
      "v_max_f32_e32 v244, v244, v253\n\t"
      "v_mov_b32_e32 v253, v244\n\t"
      "s_nop 1\n\t"
      "v_permlane16_swap_b32_e32 v244, v253\n\t"
      "s_nop 0\n\t"
      "v_max_f32_e32 v244, v244, v253\n\t"
      "v_mov_b32_e32 v253, v244\n\t"
      "s_nop 1\n\t"
      "v_permlane32_swap_b32_e32 v244, v253\n\t"
      "s_nop 0\n\t"
      "v_max_f32_e32 v244, v244, v253\n\t"
      "v_sub_f32_e32 v208, v208, v244\n\t"
      "v_sub_f32_e32 v209, v209, v244\n\t"
      "v_sub_f32_e32 v210, v210, v244\n\t"
      "v_sub_f32_e32 v211, v211, v244\n\t"
      "v_sub_f32_e32 v212, v212, v244\n\t"
      "v_sub_f32_e32 v213, v213, v244\n\t"
      "v_sub_f32_e32 v214, v214, v244\n\t"
      "v_sub_f32_e32 v215, v215, v244\n\t"
      "v_sub_f32_e32 v247, 0, v244\n\t"
      "v_sub_f32_e32 v246, 0, v244\n\t"
      "v_sub_f32_e32 v245, 0, v244\n\t"
      "v_sub_f32_e32 v244, 0, v244\n\t"
      "v_exp_f32_e32 v215, v215\n\t"
      "v_exp_f32_e32 v213, v213\n\t"
      "v_exp_f32_e32 v212, v212\n\t"
      "s_sub_u32 s30, s30, 1\n\t"
      "s_cmp_lt_u32 s30, 3\n\t"
      "s_waitcnt lgkmcnt(4)\n\t"
      "s_cbranch_scc1 L_s1_A3_%=\n\t"
      "L_dm0_A3_%=:\n\t"
      "v_mfma_f32_16x16x32_bf16 v[192:195], v[160:163], v[96:99], v[184:187]\n\t"
      "s_cmp_lt_u32 s42, s40\n\t"
      "ds_read_b64_tr_b16 v[176:177], v249 offset:0\n\t"
      "v_cvt_pk_bf16_f32 v226, v212, v213\n\t"
      "v_mfma_f32_16x16x32_bf16 v[192:195], v[164:167], v[100:103], v[192:195]\n\t"
      "s_cselect_b32 s34, s35, s58\n\t"
      "ds_read_b64_tr_b16 v[178:179], v249 offset:4096\n\t"
      "v_add_u32_e32 v248, s34, v248\n\t"
      "v_mfma_f32_16x16x32_bf16 v[192:195], v[168:171], v[104:107], v[192:195]\n\t"
      "s_mov_b32 m0, s42\n\t"
      "v_exp_f32_e32 v214, v214\n\t"
      "v_mfma_f32_16x16x32_bf16 v[192:195], v[172:175], v[108:111], v[192:195]\n\t"
      "s_waitcnt lgkmcnt(2)\n\t"
      "ds_read_b64_tr_b16 v[180:181], v250 offset:0\n\t"
      "v_cvt_pk_bf16_f32 v227, v214, v215\n\t"
      "v_mfma_f32_16x16x32_bf16 v[196:199], v[144:147], v[96:99], v[184:187]\n\t"
      "s_add_u32 s42, s42, s34\n\t"
      "ds_read_b64_tr_b16 v[182:183], v250 offset:4096\n\t"
      "v_mfma_f32_16x16x32_bf16 v[196:199], v[148:151], v[100:103], v[196:199]\n\t"
      "v_exp_f32_e32 v209, v209\n\t"
      "v_mfma_f32_16x16x32_bf16 v[196:199], v[152:155], v[104:107], v[196:199]\n\t"
      "v_exp_f32_e32 v208, v208\n\t"
      "v_mfma_f32_16x16x32_bf16 v[196:199], v[156:159], v[108:111], v[196:199]\n\t"
      "v_exp_f32_e32 v210, v210\n\t"
      "v_mfma_f32_16x16x32_bf16 v[200:203], v[160:163], v[112:115], v[188:191]\n\t"
      "v_exp_f32_e32 v194, v194\n\t"
      "v_mfma_f32_16x16x32_bf16 v[200:203], v[164:167], v[116:119], v[200:203]\n\t"
      "v_exp_f32_e32 v192, v192\n\t"
      "v_mfma_f32_16x16x32_bf16 v[200:203], v[168:171], v[120:123], v[200:203]\n\t"
      "v_exp_f32_e32 v193, v193\n\t"
      "v_mfma_f32_16x16x32_bf16 v[200:203], v[172:175], v[124:127], v[200:203]\n\t"
      "v_exp_f32_e32 v195, v195\n\t"
      "v_mfma_f32_16x16x32_bf16 v[204:207], v[144:147], v[112:115], v[188:191]\n\t"
      "v_exp_f32_e32 v196, v196\n\t"
      "v_mfma_f32_16x16x32_bf16 v[204:207], v[148:151], v[116:119], v[204:207]\n\t"
      "v_exp_f32_e32 v198, v198\n\t"
      "v_mfma_f32_16x16x32_bf16 v[204:207], v[152:155], v[120:123], v[204:207]\n\t"
      "v_exp_f32_e32 v197, v197\n\t"
      "v_mfma_f32_16x16x32_bf16 v[204:207], v[156:159], v[124:127], v[204:207]\n\t"
      "v_exp_f32_e32 v199, v199\n\t"
      "v_mfma_f32_16x16x32_bf16 v[212:215], v[144:147], v[128:131], v[244:247]\n\t"
      "ds_read_b64_tr_b16 v[144:145], v249 offset:1024\n\t"
      "v_cvt_pk_bf16_f32 v224, v208, v209\n\t"
      "v_mfma_f32_16x16x32_bf16 v[212:215], v[148:151], v[132:135], v[212:215]\n\t"
      "ds_read_b64_tr_b16 v[146:147], v249 offset:5120\n\t"
      "v_mfma_f32_16x16x32_bf16 v[212:215], v[152:155], v[136:139], v[212:215]\n\t"
      "ds_read_b64_tr_b16 v[148:149], v250 offset:1024\n\t"
      "v_exp_f32_e32 v211, v211\n\t"
      "v_mfma_f32_16x16x32_bf16 v[212:215], v[156:159], v[140:143], v[212:215]\n\t"
      "ds_read_b64_tr_b16 v[150:151], v250 offset:5120\n\t"
      "v_cvt_pk_bf16_f32 v225, v210, v211\n\t"
      "v_mfma_f32_16x16x32_bf16 v[208:211], v[160:163], v[128:131], v[244:247]\n\t"
      "ds_read_b64_tr_b16 v[152:153], v249 offset:1536\n\t"
      "v_mfma_f32_16x16x32_bf16 v[208:211], v[164:167], v[132:135], v[208:211]\n\t"
      "ds_read_b64_tr_b16 v[154:155], v249 offset:5632\n\t"
      "v_mfma_f32_16x16x32_bf16 v[208:211], v[168:171], v[136:139], v[208:211]\n\t"
      "ds_read_b64_tr_b16 v[156:157], v250 offset:1536\n\t"
      "ds_read_b64_tr_b16 v[158:159], v250 offset:5632\n\t"
      "v_mfma_f32_16x16x32_bf16 v[208:211], v[172:175], v[140:143], v[208:211]\n\t"
      "ds_read_b64_tr_b16 v[160:161], v249 offset:512\n\t"
      "v_mfma_f32_16x16x32_bf16 v[228:231], v[240:243], v[216:219], v[228:231]\n\t"
      "s_waitcnt lgkmcnt(9)\n\t"
      "ds_read_b64_tr_b16 v[162:163], v249 offset:4608\n\t"
      "ds_read_b64_tr_b16 v[164:165], v250 offset:512\n\t"
      "ds_read_b64_tr_b16 v[166:167], v250 offset:4608\n\t"
      "v_mfma_f32_16x16x32_bf16 v[0:3], v[176:179], v[216:219], v[0:3]\n\t"
      "v_add_u32_e32 v250, s34, v250\n\t"
      "v_add_u32_e32 v249, s34, v249\n\t"
      "v_mfma_f32_16x16x32_bf16 v[4:7], v[180:183], v[216:219], v[4:7]\n\t"
      "s_waitcnt lgkmcnt(8)\n\t"
      "v_exp_f32_e32 v202, v202\n\t"
      "v_mfma_f32_16x16x32_bf16 v[16:19], v[144:147], v[216:219], v[16:19]\n\t"
      "v_exp_f32_e32 v203, v203\n\t"
      "v_mfma_f32_16x16x32_bf16 v[20:23], v[148:151], v[216:219], v[20:23]\n\t"
      "s_waitcnt lgkmcnt(4)\n\t"
      "v_exp_f32_e32 v200, v200\n\t"
      "v_mfma_f32_16x16x32_bf16 v[24:27], v[152:155], v[216:219], v[24:27]\n\t"
      "v_exp_f32_e32 v201, v201\n\t"
      "v_mfma_f32_16x16x32_bf16 v[28:31], v[156:159], v[216:219], v[28:31]\n\t"
      "s_sleep 2\n\t"
      "s_waitcnt vmcnt(4) lgkmcnt(0)\n\t"
      "s_barrier\n\t"
      "v_mfma_f32_16x16x32_bf16 v[8:11], v[160:163], v[216:219], v[8:11]\n\t"
      "s_sub_u32 s30, s30, 1\n\t"
      "v_exp_f32_e32 v206, v206\n\t"
      "v_mfma_f32_16x16x32_bf16 v[12:15], v[164:167], v[216:219], v[12:15]\n\t"
      "buffer_load_dwordx4 v251, s[12:15], s31 offen lds\n\t"
      "v_cvt_pk_bf16_f32 v217, v194, v195\n\t"
      "v_mfma_f32_16x16x32_bf16 v[232:235], v[240:243], v[220:223], v[232:235]\n\t"
      "s_addk_i32 m0, 896\n\t"
      "ds_read_b128 v[168:171], v248 offset:1024\n\t"
      "v_cvt_pk_bf16_f32 v216, v192, v193\n\t"
      "v_mfma_f32_16x16x32_bf16 v[32:35], v[176:179], v[220:223], v[32:35]\n\t"
      "buffer_load_dwordx4 v251, s[12:15], s31 offen offset:128 lds\n\t"
      "v_cvt_pk_bf16_f32 v219, v198, v199\n\t"
      "v_mfma_f32_16x16x32_bf16 v[36:39], v[180:183], v[220:223], v[36:39]\n\t"
      "s_addk_i32 m0, 7296\n\t"
      "ds_read_b128 v[172:175], v248 offset:1536\n\t"
      "v_cvt_pk_bf16_f32 v218, v196, v197\n\t"
      "v_mfma_f32_16x16x32_bf16 v[48:51], v[144:147], v[220:223], v[48:51]\n\t"
      "buffer_load_dwordx4 v252, s[16:19], s41 offen lds\n\t"
      "v_mfma_f32_16x16x32_bf16 v[52:55], v[148:151], v[220:223], v[52:55]\n\t"
      "s_addk_i32 m0, 896\n\t"
      "v_exp_f32_e32 v207, v207\n\t"
      "v_mfma_f32_16x16x32_bf16 v[56:59], v[152:155], v[220:223], v[56:59]\n\t"
      "buffer_load_dwordx4 v252, s[16:19], s41 offen offset:128 lds\n\t"
      "v_mfma_f32_16x16x32_bf16 v[60:63], v[156:159], v[220:223], v[60:63]\n\t"
      "s_mov_b32 s41, s31\n\t"
      "v_exp_f32_e32 v204, v204\n\t"
      "v_mfma_f32_16x16x32_bf16 v[40:43], v[160:163], v[220:223], v[40:43]\n\t"
      "s_add_u32 s31, s31, 0x20000\n\t"
      "v_exp_f32_e32 v205, v205\n\t"
      "v_mfma_f32_16x16x32_bf16 v[44:47], v[164:167], v[220:223], v[44:47]\n\t"
      "v_cvt_pk_bf16_f32 v221, v202, v203\n\t"
      "v_cvt_pk_bf16_f32 v220, v200, v201\n\t"
      "v_mfma_f32_16x16x32_bf16 v[72:75], v[160:163], v[224:227], v[72:75]\n\t"
      "ds_read_b128 v[160:163], v248 offset:0\n\t"
      "v_cvt_pk_bf16_f32 v223, v206, v207\n\t"
      "v_mfma_f32_16x16x32_bf16 v[76:79], v[164:167], v[224:227], v[76:79]\n\t"
      "ds_read_b128 v[164:167], v248 offset:512\n\t"
      "v_cvt_pk_bf16_f32 v222, v204, v205\n\t"
      "v_mfma_f32_16x16x32_bf16 v[80:83], v[144:147], v[224:227], v[80:83]\n\t"
      "ds_read_b128 v[144:147], v248 offset:4096\n\t"
      "v_mfma_f32_16x16x32_bf16 v[84:87], v[148:151], v[224:227], v[84:87]\n\t"
      "v_exp_f32_e32 v215, v215\n\t"
      "v_mfma_f32_16x16x32_bf16 v[88:91], v[152:155], v[224:227], v[88:91]\n\t"
      "v_exp_f32_e32 v213, v213\n\t"
      "v_mfma_f32_16x16x32_bf16 v[92:95], v[156:159], v[224:227], v[92:95]\n\t"
      "ds_read_b128 v[148:151], v248 offset:4608\n\t"
      "v_mfma_f32_16x16x32_bf16 v[64:67], v[176:179], v[224:227], v[64:67]\n\t"
      "ds_read_b128 v[152:155], v248 offset:5120\n\t"
      "ds_read_b128 v[156:159], v248 offset:5632\n\t"
      "v_mfma_f32_16x16x32_bf16 v[68:71], v[180:183], v[224:227], v[68:71]\n\t"
      "s_cmp_ge_u32 s30, s25\n\t"
      "v_exp_f32_e32 v212, v212\n\t"
      "v_mfma_f32_16x16x32_bf16 v[236:239], v[240:243], v[224:227], v[236:239]\n\t"
      "s_waitcnt lgkmcnt(4)\n\t"
      "s_cbranch_scc1 L_dm0_A3_%=\n\t"
      "s_cmp_lt_u32 s30, 3\n\t"
      "s_cbranch_scc1 L_s1_A3_%=\n\t"
      "L_dma_A3_%=:\n\t"
      "v_mfma_f32_16x16x32_bf16 v[192:195], v[160:163], v[96:99], v[184:187]\n\t"
      "s_cmp_lt_u32 s42, s40\n\t"
      "ds_read_b64_tr_b16 v[176:177], v249 offset:0\n\t"
      "v_cvt_pk_bf16_f32 v226, v212, v213\n\t"
      "v_mfma_f32_16x16x32_bf16 v[192:195], v[164:167], v[100:103], v[192:195]\n\t"
      "s_cselect_b32 s34, s35, s58\n\t"
      "ds_read_b64_tr_b16 v[178:179], v249 offset:4096\n\t"
      "v_add_u32_e32 v248, s34, v248\n\t"
      "v_mfma_f32_16x16x32_bf16 v[192:195], v[168:171], v[104:107], v[192:195]\n\t"
      "s_mov_b32 m0, s42\n\t"
      "v_exp_f32_e32 v214, v214\n\t"
      "v_mfma_f32_16x16x32_bf16 v[192:195], v[172:175], v[108:111], v[192:195]\n\t"
      "s_waitcnt lgkmcnt(2)\n\t"
      "ds_read_b64_tr_b16 v[180:181], v250 offset:0\n\t"
      "v_cvt_pk_bf16_f32 v227, v214, v215\n\t"
      "v_mfma_f32_16x16x32_bf16 v[196:199], v[144:147], v[96:99], v[184:187]\n\t"
      "s_add_u32 s42, s42, s34\n\t"
      "ds_read_b64_tr_b16 v[182:183], v250 offset:4096\n\t"
      "v_mfma_f32_16x16x32_bf16 v[196:199], v[148:151], v[100:103], v[196:199]\n\t"
      "v_exp_f32_e32 v209, v209\n\t"
      "v_mfma_f32_16x16x32_bf16 v[196:199], v[152:155], v[104:107], v[196:199]\n\t"
      "v_exp_f32_e32 v208, v208\n\t"
      "v_mfma_f32_16x16x32_bf16 v[196:199], v[156:159], v[108:111], v[196:199]\n\t"
      "v_exp_f32_e32 v210, v210\n\t"
      "v_mfma_f32_16x16x32_bf16 v[200:203], v[160:163], v[112:115], v[188:191]\n\t"
      "v_exp_f32_e32 v194, v194\n\t"
      "v_mfma_f32_16x16x32_bf16 v[200:203], v[164:167], v[116:119], v[200:203]\n\t"
      "v_exp_f32_e32 v192, v192\n\t"
      "v_mfma_f32_16x16x32_bf16 v[200:203], v[168:171], v[120:123], v[200:203]\n\t"
      "v_exp_f32_e32 v193, v193\n\t"
      "v_mfma_f32_16x16x32_bf16 v[200:203], v[172:175], v[124:127], v[200:203]\n\t"
      "v_exp_f32_e32 v195, v195\n\t"
      "v_mfma_f32_16x16x32_bf16 v[204:207], v[144:147], v[112:115], v[188:191]\n\t"
      "v_exp_f32_e32 v196, v196\n\t"
      "v_mfma_f32_16x16x32_bf16 v[204:207], v[148:151], v[116:119], v[204:207]\n\t"
      "v_exp_f32_e32 v198, v198\n\t"
      "v_mfma_f32_16x16x32_bf16 v[204:207], v[152:155], v[120:123], v[204:207]\n\t"
      "v_exp_f32_e32 v197, v197\n\t"
      "v_mfma_f32_16x16x32_bf16 v[204:207], v[156:159], v[124:127], v[204:207]\n\t"
      "v_exp_f32_e32 v199, v199\n\t"
      "v_mfma_f32_16x16x32_bf16 v[212:215], v[144:147], v[128:131], v[244:247]\n\t"
      "ds_read_b64_tr_b16 v[144:145], v249 offset:1024\n\t"
      "v_cvt_pk_bf16_f32 v224, v208, v209\n\t"
      "v_mfma_f32_16x16x32_bf16 v[212:215], v[148:151], v[132:135], v[212:215]\n\t"
      "ds_read_b64_tr_b16 v[146:147], v249 offset:5120\n\t"
      "v_mfma_f32_16x16x32_bf16 v[212:215], v[152:155], v[136:139], v[212:215]\n\t"
      "ds_read_b64_tr_b16 v[148:149], v250 offset:1024\n\t"
      "v_exp_f32_e32 v211, v211\n\t"
      "v_mfma_f32_16x16x32_bf16 v[212:215], v[156:159], v[140:143], v[212:215]\n\t"
      "ds_read_b64_tr_b16 v[150:151], v250 offset:5120\n\t"
      "v_cvt_pk_bf16_f32 v225, v210, v211\n\t"
      "v_mfma_f32_16x16x32_bf16 v[208:211], v[160:163], v[128:131], v[244:247]\n\t"
      "ds_read_b64_tr_b16 v[152:153], v249 offset:1536\n\t"
      "v_mfma_f32_16x16x32_bf16 v[208:211], v[164:167], v[132:135], v[208:211]\n\t"
      "ds_read_b64_tr_b16 v[154:155], v249 offset:5632\n\t"
      "v_mfma_f32_16x16x32_bf16 v[208:211], v[168:171], v[136:139], v[208:211]\n\t"
      "ds_read_b64_tr_b16 v[156:157], v250 offset:1536\n\t"
      "ds_read_b64_tr_b16 v[158:159], v250 offset:5632\n\t"
      "v_mfma_f32_16x16x32_bf16 v[208:211], v[172:175], v[140:143], v[208:211]\n\t"
      "ds_read_b64_tr_b16 v[160:161], v249 offset:512\n\t"
      "v_mfma_f32_16x16x32_bf16 v[228:231], v[240:243], v[216:219], v[228:231]\n\t"
      "s_waitcnt lgkmcnt(9)\n\t"
      "ds_read_b64_tr_b16 v[162:163], v249 offset:4608\n\t"
      "ds_read_b64_tr_b16 v[164:165], v250 offset:512\n\t"
      "ds_read_b64_tr_b16 v[166:167], v250 offset:4608\n\t"
      "v_mfma_f32_16x16x32_bf16 v[0:3], v[176:179], v[216:219], v[0:3]\n\t"
      "v_add_u32_e32 v250, s34, v250\n\t"
      "v_add_u32_e32 v249, s34, v249\n\t"
      "v_mfma_f32_16x16x32_bf16 v[4:7], v[180:183], v[216:219], v[4:7]\n\t"
      "s_waitcnt lgkmcnt(8)\n\t"
      "v_exp_f32_e32 v202, v202\n\t"
      "v_mfma_f32_16x16x32_bf16 v[16:19], v[144:147], v[216:219], v[16:19]\n\t"
      "v_exp_f32_e32 v203, v203\n\t"
      "v_mfma_f32_16x16x32_bf16 v[20:23], v[148:151], v[216:219], v[20:23]\n\t"
      "s_waitcnt lgkmcnt(4)\n\t"
      "v_exp_f32_e32 v200, v200\n\t"
      "v_mfma_f32_16x16x32_bf16 v[24:27], v[152:155], v[216:219], v[24:27]\n\t"
      "v_exp_f32_e32 v201, v201\n\t"
      "v_mfma_f32_16x16x32_bf16 v[28:31], v[156:159], v[216:219], v[28:31]\n\t"
      "s_waitcnt vmcnt(4) lgkmcnt(0)\n\t"
      "s_barrier\n\t"
      "v_mfma_f32_16x16x32_bf16 v[8:11], v[160:163], v[216:219], v[8:11]\n\t"
      "s_sub_u32 s30, s30, 1\n\t"
      "v_exp_f32_e32 v206, v206\n\t"
      "v_mfma_f32_16x16x32_bf16 v[12:15], v[164:167], v[216:219], v[12:15]\n\t"
      "buffer_load_dwordx4 v251, s[12:15], s31 offen lds\n\t"
      "v_cvt_pk_bf16_f32 v217, v194, v195\n\t"
      "v_mfma_f32_16x16x32_bf16 v[232:235], v[240:243], v[220:223], v[232:235]\n\t"
      "s_addk_i32 m0, 896\n\t"
      "ds_read_b128 v[168:171], v248 offset:1024\n\t"
      "v_cvt_pk_bf16_f32 v216, v192, v193\n\t"
      "v_mfma_f32_16x16x32_bf16 v[32:35], v[176:179], v[220:223], v[32:35]\n\t"
      "buffer_load_dwordx4 v251, s[12:15], s31 offen offset:128 lds\n\t"
      "v_cvt_pk_bf16_f32 v219, v198, v199\n\t"
      "v_mfma_f32_16x16x32_bf16 v[36:39], v[180:183], v[220:223], v[36:39]\n\t"
      "s_addk_i32 m0, 7296\n\t"
      "ds_read_b128 v[172:175], v248 offset:1536\n\t"
      "v_cvt_pk_bf16_f32 v218, v196, v197\n\t"
      "v_mfma_f32_16x16x32_bf16 v[48:51], v[144:147], v[220:223], v[48:51]\n\t"
      "buffer_load_dwordx4 v252, s[16:19], s41 offen lds\n\t"
      "v_mfma_f32_16x16x32_bf16 v[52:55], v[148:151], v[220:223], v[52:55]\n\t"
      "s_addk_i32 m0, 896\n\t"
      "v_exp_f32_e32 v207, v207\n\t"
      "v_mfma_f32_16x16x32_bf16 v[56:59], v[152:155], v[220:223], v[56:59]\n\t"
      "buffer_load_dwordx4 v252, s[16:19], s41 offen offset:128 lds\n\t"
      "v_mfma_f32_16x16x32_bf16 v[60:63], v[156:159], v[220:223], v[60:63]\n\t"
      "s_mov_b32 s41, s31\n\t"
      "v_exp_f32_e32 v204, v204\n\t"
      "v_mfma_f32_16x16x32_bf16 v[40:43], v[160:163], v[220:223], v[40:43]\n\t"
      "s_add_u32 s31, s31, 0x20000\n\t"
      "v_exp_f32_e32 v205, v205\n\t"
      "v_mfma_f32_16x16x32_bf16 v[44:47], v[164:167], v[220:223], v[44:47]\n\t"
      "v_cvt_pk_bf16_f32 v221, v202, v203\n\t"
      "v_cvt_pk_bf16_f32 v220, v200, v201\n\t"
      "v_mfma_f32_16x16x32_bf16 v[72:75], v[160:163], v[224:227], v[72:75]\n\t"
      "ds_read_b128 v[160:163], v248 offset:0\n\t"
      "v_cvt_pk_bf16_f32 v223, v206, v207\n\t"
      "v_mfma_f32_16x16x32_bf16 v[76:79], v[164:167], v[224:227], v[76:79]\n\t"
      "ds_read_b128 v[164:167], v248 offset:512\n\t"
      "v_cvt_pk_bf16_f32 v222, v204, v205\n\t"
      "v_mfma_f32_16x16x32_bf16 v[80:83], v[144:147], v[224:227], v[80:83]\n\t"
      "ds_read_b128 v[144:147], v248 offset:4096\n\t"
      "v_mfma_f32_16x16x32_bf16 v[84:87], v[148:151], v[224:227], v[84:87]\n\t"
      "v_exp_f32_e32 v215, v215\n\t"
      "v_mfma_f32_16x16x32_bf16 v[88:91], v[152:155], v[224:227], v[88:91]\n\t"
      "v_exp_f32_e32 v213, v213\n\t"
      "v_mfma_f32_16x16x32_bf16 v[92:95], v[156:159], v[224:227], v[92:95]\n\t"
      "ds_read_b128 v[148:151], v248 offset:4608\n\t"
      "v_mfma_f32_16x16x32_bf16 v[64:67], v[176:179], v[224:227], v[64:67]\n\t"
      "ds_read_b128 v[152:155], v248 offset:5120\n\t"
      "ds_read_b128 v[156:159], v248 offset:5632\n\t"
      "v_mfma_f32_16x16x32_bf16 v[68:71], v[180:183], v[224:227], v[68:71]\n\t"
      "s_cmp_ge_u32 s30, s59\n\t"
      "v_exp_f32_e32 v212, v212\n\t"
      "v_mfma_f32_16x16x32_bf16 v[236:239], v[240:243], v[224:227], v[236:239]\n\t"
      "s_waitcnt lgkmcnt(4)\n\t"
      "s_cbranch_scc0 L_s1_A3_%=\n\t"
      "v_mfma_f32_16x16x32_bf16 v[192:195], v[160:163], v[96:99], v[184:187]\n\t"
      "s_cmp_lt_u32 s42, s40\n\t"
      "ds_read_b64_tr_b16 v[176:177], v249 offset:0\n\t"
      "v_cvt_pk_bf16_f32 v226, v212, v213\n\t"
      "v_mfma_f32_16x16x32_bf16 v[192:195], v[164:167], v[100:103], v[192:195]\n\t"
      "s_cselect_b32 s34, s35, s58\n\t"
      "ds_read_b64_tr_b16 v[178:179], v249 offset:4096\n\t"
      "v_add_u32_e32 v248, s34, v248\n\t"
      "v_mfma_f32_16x16x32_bf16 v[192:195], v[168:171], v[104:107], v[192:195]\n\t"
      "s_mov_b32 m0, s42\n\t"
      "v_exp_f32_e32 v214, v214\n\t"
      "v_mfma_f32_16x16x32_bf16 v[192:195], v[172:175], v[108:111], v[192:195]\n\t"
      "s_waitcnt lgkmcnt(2)\n\t"
      "ds_read_b64_tr_b16 v[180:181], v250 offset:0\n\t"
      "v_cvt_pk_bf16_f32 v227, v214, v215\n\t"
      "v_mfma_f32_16x16x32_bf16 v[196:199], v[144:147], v[96:99], v[184:187]\n\t"
      "s_add_u32 s42, s42, s34\n\t"
      "ds_read_b64_tr_b16 v[182:183], v250 offset:4096\n\t"
      "v_mfma_f32_16x16x32_bf16 v[196:199], v[148:151], v[100:103], v[196:199]\n\t"
      "v_exp_f32_e32 v209, v209\n\t"
      "v_mfma_f32_16x16x32_bf16 v[196:199], v[152:155], v[104:107], v[196:199]\n\t"
      "v_exp_f32_e32 v208, v208\n\t"
      "v_mfma_f32_16x16x32_bf16 v[196:199], v[156:159], v[108:111], v[196:199]\n\t"
      "v_exp_f32_e32 v210, v210\n\t"
      "v_mfma_f32_16x16x32_bf16 v[200:203], v[160:163], v[112:115], v[188:191]\n\t"
      "v_exp_f32_e32 v194, v194\n\t"
      "v_mfma_f32_16x16x32_bf16 v[200:203], v[164:167], v[116:119], v[200:203]\n\t"
      "v_exp_f32_e32 v192, v192\n\t"
      "v_mfma_f32_16x16x32_bf16 v[200:203], v[168:171], v[120:123], v[200:203]\n\t"
      "v_exp_f32_e32 v193, v193\n\t"
      "v_mfma_f32_16x16x32_bf16 v[200:203], v[172:175], v[124:127], v[200:203]\n\t"
      "v_exp_f32_e32 v195, v195\n\t"
      "v_mfma_f32_16x16x32_bf16 v[204:207], v[144:147], v[112:115], v[188:191]\n\t"
      "v_exp_f32_e32 v196, v196\n\t"
      "v_mfma_f32_16x16x32_bf16 v[204:207], v[148:151], v[116:119], v[204:207]\n\t"
      "v_exp_f32_e32 v198, v198\n\t"
      "v_mfma_f32_16x16x32_bf16 v[204:207], v[152:155], v[120:123], v[204:207]\n\t"
      "v_exp_f32_e32 v197, v197\n\t"
      "v_mfma_f32_16x16x32_bf16 v[204:207], v[156:159], v[124:127], v[204:207]\n\t"
      "v_exp_f32_e32 v199, v199\n\t"
      "v_mfma_f32_16x16x32_bf16 v[212:215], v[144:147], v[128:131], v[244:247]\n\t"
      "ds_read_b64_tr_b16 v[144:145], v249 offset:1024\n\t"
      "v_cvt_pk_bf16_f32 v224, v208, v209\n\t"
      "v_mfma_f32_16x16x32_bf16 v[212:215], v[148:151], v[132:135], v[212:215]\n\t"
      "ds_read_b64_tr_b16 v[146:147], v249 offset:5120\n\t"
      "v_mfma_f32_16x16x32_bf16 v[212:215], v[152:155], v[136:139], v[212:215]\n\t"
      "ds_read_b64_tr_b16 v[148:149], v250 offset:1024\n\t"
      "v_exp_f32_e32 v211, v211\n\t"
      "v_mfma_f32_16x16x32_bf16 v[212:215], v[156:159], v[140:143], v[212:215]\n\t"
      "ds_read_b64_tr_b16 v[150:151], v250 offset:5120\n\t"
      "v_cvt_pk_bf16_f32 v225, v210, v211\n\t"
      "v_mfma_f32_16x16x32_bf16 v[208:211], v[160:163], v[128:131], v[244:247]\n\t"
      "ds_read_b64_tr_b16 v[152:153], v249 offset:1536\n\t"
      "v_mfma_f32_16x16x32_bf16 v[208:211], v[164:167], v[132:135], v[208:211]\n\t"
      "ds_read_b64_tr_b16 v[154:155], v249 offset:5632\n\t"
      "v_mfma_f32_16x16x32_bf16 v[208:211], v[168:171], v[136:139], v[208:211]\n\t"
      "ds_read_b64_tr_b16 v[156:157], v250 offset:1536\n\t"
      "ds_read_b64_tr_b16 v[158:159], v250 offset:5632\n\t"
      "v_mfma_f32_16x16x32_bf16 v[208:211], v[172:175], v[140:143], v[208:211]\n\t"
      "ds_read_b64_tr_b16 v[160:161], v249 offset:512\n\t"
      "v_mfma_f32_16x16x32_bf16 v[228:231], v[240:243], v[216:219], v[228:231]\n\t"
      "s_waitcnt lgkmcnt(9)\n\t"
      "ds_read_b64_tr_b16 v[162:163], v249 offset:4608\n\t"
      "ds_read_b64_tr_b16 v[164:165], v250 offset:512\n\t"
      "ds_read_b64_tr_b16 v[166:167], v250 offset:4608\n\t"
      "v_mfma_f32_16x16x32_bf16 v[0:3], v[176:179], v[216:219], v[0:3]\n\t"
      "v_add_u32_e32 v250, s34, v250\n\t"
      "v_add_u32_e32 v249, s34, v249\n\t"
      "v_mfma_f32_16x16x32_bf16 v[4:7], v[180:183], v[216:219], v[4:7]\n\t"
      "s_waitcnt lgkmcnt(8)\n\t"
      "v_exp_f32_e32 v202, v202\n\t"
      "v_mfma_f32_16x16x32_bf16 v[16:19], v[144:147], v[216:219], v[16:19]\n\t"
      "v_exp_f32_e32 v203, v203\n\t"
      "v_mfma_f32_16x16x32_bf16 v[20:23], v[148:151], v[216:219], v[20:23]\n\t"
      "s_waitcnt lgkmcnt(4)\n\t"
      "v_exp_f32_e32 v200, v200\n\t"
      "v_mfma_f32_16x16x32_bf16 v[24:27], v[152:155], v[216:219], v[24:27]\n\t"
      "v_exp_f32_e32 v201, v201\n\t"
      "v_mfma_f32_16x16x32_bf16 v[28:31], v[156:159], v[216:219], v[28:31]\n\t"
      "s_waitcnt vmcnt(4) lgkmcnt(0)\n\t"
      "s_barrier\n\t"
      "v_mfma_f32_16x16x32_bf16 v[8:11], v[160:163], v[216:219], v[8:11]\n\t"
      "s_sub_u32 s30, s30, 1\n\t"
      "v_exp_f32_e32 v206, v206\n\t"
      "v_mfma_f32_16x16x32_bf16 v[12:15], v[164:167], v[216:219], v[12:15]\n\t"
      "buffer_load_dwordx4 v251, s[12:15], s31 offen lds\n\t"
      "v_cvt_pk_bf16_f32 v217, v194, v195\n\t"
      "v_mfma_f32_16x16x32_bf16 v[232:235], v[240:243], v[220:223], v[232:235]\n\t"
      "s_addk_i32 m0, 896\n\t"
      "ds_read_b128 v[168:171], v248 offset:1024\n\t"
      "v_cvt_pk_bf16_f32 v216, v192, v193\n\t"
      "v_mfma_f32_16x16x32_bf16 v[32:35], v[176:179], v[220:223], v[32:35]\n\t"
      "buffer_load_dwordx4 v251, s[12:15], s31 offen offset:128 lds\n\t"
      "v_cvt_pk_bf16_f32 v219, v198, v199\n\t"
      "v_mfma_f32_16x16x32_bf16 v[36:39], v[180:183], v[220:223], v[36:39]\n\t"
      "s_addk_i32 m0, 7296\n\t"
      "ds_read_b128 v[172:175], v248 offset:1536\n\t"
      "v_cvt_pk_bf16_f32 v218, v196, v197\n\t"
      "v_mfma_f32_16x16x32_bf16 v[48:51], v[144:147], v[220:223], v[48:51]\n\t"
      "buffer_load_dwordx4 v252, s[16:19], s41 offen lds\n\t"
      "v_mfma_f32_16x16x32_bf16 v[52:55], v[148:151], v[220:223], v[52:55]\n\t"
      "s_addk_i32 m0, 896\n\t"
      "v_exp_f32_e32 v207, v207\n\t"
      "v_mfma_f32_16x16x32_bf16 v[56:59], v[152:155], v[220:223], v[56:59]\n\t"
      "buffer_load_dwordx4 v252, s[16:19], s41 offen offset:128 lds\n\t"
      "v_mfma_f32_16x16x32_bf16 v[60:63], v[156:159], v[220:223], v[60:63]\n\t"
      "s_mov_b32 s41, s31\n\t"
      "v_exp_f32_e32 v204, v204\n\t"
      "v_mfma_f32_16x16x32_bf16 v[40:43], v[160:163], v[220:223], v[40:43]\n\t"
      "s_add_u32 s31, s31, 0x20000\n\t"
      "v_exp_f32_e32 v205, v205\n\t"
      "v_mfma_f32_16x16x32_bf16 v[44:47], v[164:167], v[220:223], v[44:47]\n\t"
      "v_cvt_pk_bf16_f32 v221, v202, v203\n\t"
      "v_cvt_pk_bf16_f32 v220, v200, v201\n\t"
      "v_mfma_f32_16x16x32_bf16 v[72:75], v[160:163], v[224:227], v[72:75]\n\t"
      "ds_read_b128 v[160:163], v248 offset:0\n\t"
      "v_cvt_pk_bf16_f32 v223, v206, v207\n\t"
      "v_mfma_f32_16x16x32_bf16 v[76:79], v[164:167], v[224:227], v[76:79]\n\t"
      "ds_read_b128 v[164:167], v248 offset:512\n\t"
      "v_cvt_pk_bf16_f32 v222, v204, v205\n\t"
      "v_mfma_f32_16x16x32_bf16 v[80:83], v[144:147], v[224:227], v[80:83]\n\t"
      "ds_read_b128 v[144:147], v248 offset:4096\n\t"
      "v_mfma_f32_16x16x32_bf16 v[84:87], v[148:151], v[224:227], v[84:87]\n\t"
      "v_exp_f32_e32 v215, v215\n\t"
      "v_mfma_f32_16x16x32_bf16 v[88:91], v[152:155], v[224:227], v[88:91]\n\t"
      "v_exp_f32_e32 v213, v213\n\t"
      "v_mfma_f32_16x16x32_bf16 v[92:95], v[156:159], v[224:227], v[92:95]\n\t"
      "ds_read_b128 v[148:151], v248 offset:4608\n\t"
      "v_mfma_f32_16x16x32_bf16 v[64:67], v[176:179], v[224:227], v[64:67]\n\t"
      "ds_read_b128 v[152:155], v248 offset:5120\n\t"
      "ds_read_b128 v[156:159], v248 offset:5632\n\t"
      "v_mfma_f32_16x16x32_bf16 v[68:71], v[180:183], v[224:227], v[68:71]\n\t"
      "s_cmp_ge_u32 s30, s59\n\t"
      "v_exp_f32_e32 v212, v212\n\t"
      "v_mfma_f32_16x16x32_bf16 v[236:239], v[240:243], v[224:227], v[236:239]\n\t"
      "s_waitcnt lgkmcnt(4)\n\t"
      "s_cbranch_scc1 L_dma_A3_%=\n\t"
      "L_s1_A3_%=:\n\t"
      "v_mbcnt_lo_u32_b32 v252, -1, 0\n\t"
      "v_mbcnt_hi_u32_b32 v252, -1, v252\n\t"
      "v_and_b32_e32 v253, 7, v252\n\t"
      "v_bfe_u32 v251, v252, 3, 1\n\t"
      "v_lshl_or_b32 v253, v253, 1, v251\n\t"
      "v_lshrrev_b32_e32 v251, 4, v252\n\t"
      "v_xor_b32_e32 v253, v253, v251\n\t"
      "v_and_b32_e32 v251, 15, v252\n\t"
      "v_lshlrev_b32_e32 v251, 4, v251\n\t"
      "v_add_lshl_u32 v253, v253, v251, 3\n\t"
      "v_lshrrev_b32_e32 v251, 3, v252\n\t"
      "s_movk_i32 s37, 0xf8\n\t"
      "v_mad_u32_u24 v254, v251, s37, v252\n\t"
      "v_xor_b32_e32 v251, v251, v252\n\t"
      "v_lshlrev_b32_e32 v251, 4, v251\n\t"
      "v_lshlrev_b32_e32 v252, 4, v254\n\t"
      "s_cmp_eq_u32 s30, 0\n\t"
      "s_cbranch_scc1 L_dr_A3_%=\n\t"
      "L_nod_A3_%=:\n\t"
      "v_mfma_f32_16x16x32_bf16 v[192:195], v[160:163], v[96:99], v[184:187]\n\t"
      "s_cmp_lt_u32 s42, s40\n\t"
      "ds_read_b64_tr_b16 v[176:177], v249 offset:0\n\t"
      "v_cvt_pk_bf16_f32 v226, v212, v213\n\t"
      "v_mfma_f32_16x16x32_bf16 v[192:195], v[164:167], v[100:103], v[192:195]\n\t"
      "s_cselect_b32 s34, s35, s58\n\t"
      "ds_read_b64_tr_b16 v[178:179], v249 offset:4096\n\t"
      "v_add_u32_e32 v248, s34, v248\n\t"
      "v_mfma_f32_16x16x32_bf16 v[192:195], v[168:171], v[104:107], v[192:195]\n\t"
      "s_mov_b32 m0, s42\n\t"
      "v_exp_f32_e32 v214, v214\n\t"
      "v_mfma_f32_16x16x32_bf16 v[192:195], v[172:175], v[108:111], v[192:195]\n\t"
      "s_waitcnt lgkmcnt(2)\n\t"
      "ds_read_b64_tr_b16 v[180:181], v250 offset:0\n\t"
      "v_cvt_pk_bf16_f32 v227, v214, v215\n\t"
      "v_mfma_f32_16x16x32_bf16 v[196:199], v[144:147], v[96:99], v[184:187]\n\t"
      "s_add_u32 s42, s42, s34\n\t"
      "ds_read_b64_tr_b16 v[182:183], v250 offset:4096\n\t"
      "v_mfma_f32_16x16x32_bf16 v[196:199], v[148:151], v[100:103], v[196:199]\n\t"
      "v_exp_f32_e32 v209, v209\n\t"
      "v_mfma_f32_16x16x32_bf16 v[196:199], v[152:155], v[104:107], v[196:199]\n\t"
      "v_exp_f32_e32 v208, v208\n\t"
      "v_mfma_f32_16x16x32_bf16 v[196:199], v[156:159], v[108:111], v[196:199]\n\t"
      "v_exp_f32_e32 v210, v210\n\t"
      "v_mfma_f32_16x16x32_bf16 v[200:203], v[160:163], v[112:115], v[188:191]\n\t"
      "v_exp_f32_e32 v194, v194\n\t"
      "v_mfma_f32_16x16x32_bf16 v[200:203], v[164:167], v[116:119], v[200:203]\n\t"
      "v_exp_f32_e32 v192, v192\n\t"
      "v_mfma_f32_16x16x32_bf16 v[200:203], v[168:171], v[120:123], v[200:203]\n\t"
      "v_exp_f32_e32 v193, v193\n\t"
      "v_mfma_f32_16x16x32_bf16 v[200:203], v[172:175], v[124:127], v[200:203]\n\t"
      "v_exp_f32_e32 v195, v195\n\t"
      "v_mfma_f32_16x16x32_bf16 v[204:207], v[144:147], v[112:115], v[188:191]\n\t"
      "v_exp_f32_e32 v196, v196\n\t"
      "v_mfma_f32_16x16x32_bf16 v[204:207], v[148:151], v[116:119], v[204:207]\n\t"
      "v_exp_f32_e32 v198, v198\n\t"
      "v_mfma_f32_16x16x32_bf16 v[204:207], v[152:155], v[120:123], v[204:207]\n\t"
      "v_exp_f32_e32 v197, v197\n\t"
      "v_mfma_f32_16x16x32_bf16 v[204:207], v[156:159], v[124:127], v[204:207]\n\t"
      "v_exp_f32_e32 v199, v199\n\t"
      "v_mfma_f32_16x16x32_bf16 v[212:215], v[144:147], v[128:131], v[244:247]\n\t"
      "ds_read_b64_tr_b16 v[144:145], v249 offset:1024\n\t"
      "v_cvt_pk_bf16_f32 v224, v208, v209\n\t"
      "v_mfma_f32_16x16x32_bf16 v[212:215], v[148:151], v[132:135], v[212:215]\n\t"
      "ds_read_b64_tr_b16 v[146:147], v249 offset:5120\n\t"
      "v_mfma_f32_16x16x32_bf16 v[212:215], v[152:155], v[136:139], v[212:215]\n\t"
      "ds_read_b64_tr_b16 v[148:149], v250 offset:1024\n\t"
      "v_exp_f32_e32 v211, v211\n\t"
      "v_mfma_f32_16x16x32_bf16 v[212:215], v[156:159], v[140:143], v[212:215]\n\t"
      "ds_read_b64_tr_b16 v[150:151], v250 offset:5120\n\t"
      "v_cvt_pk_bf16_f32 v225, v210, v211\n\t"
      "v_mfma_f32_16x16x32_bf16 v[208:211], v[160:163], v[128:131], v[244:247]\n\t"
      "ds_read_b64_tr_b16 v[152:153], v249 offset:1536\n\t"
      "v_mfma_f32_16x16x32_bf16 v[208:211], v[164:167], v[132:135], v[208:211]\n\t"
      "ds_read_b64_tr_b16 v[154:155], v249 offset:5632\n\t"
      "v_mfma_f32_16x16x32_bf16 v[208:211], v[168:171], v[136:139], v[208:211]\n\t"
      "ds_read_b64_tr_b16 v[156:157], v250 offset:1536\n\t"
      "ds_read_b64_tr_b16 v[158:159], v250 offset:5632\n\t"
      "v_mfma_f32_16x16x32_bf16 v[208:211], v[172:175], v[140:143], v[208:211]\n\t"
      "ds_read_b64_tr_b16 v[160:161], v249 offset:512\n\t"
      "v_mfma_f32_16x16x32_bf16 v[228:231], v[240:243], v[216:219], v[228:231]\n\t"
      "s_waitcnt lgkmcnt(9)\n\t"
      "ds_read_b64_tr_b16 v[162:163], v249 offset:4608\n\t"
      "ds_read_b64_tr_b16 v[164:165], v250 offset:512\n\t"
      "ds_read_b64_tr_b16 v[166:167], v250 offset:4608\n\t"
      "v_mfma_f32_16x16x32_bf16 v[0:3], v[176:179], v[216:219], v[0:3]\n\t"
      "v_add_u32_e32 v250, s34, v250\n\t"
      "v_add_u32_e32 v249, s34, v249\n\t"
      "v_mfma_f32_16x16x32_bf16 v[4:7], v[180:183], v[216:219], v[4:7]\n\t"
      "s_waitcnt lgkmcnt(8)\n\t"
      "v_exp_f32_e32 v202, v202\n\t"
      "v_mfma_f32_16x16x32_bf16 v[16:19], v[144:147], v[216:219], v[16:19]\n\t"
      "v_exp_f32_e32 v203, v203\n\t"
      "v_mfma_f32_16x16x32_bf16 v[20:23], v[148:151], v[216:219], v[20:23]\n\t"
      "s_waitcnt lgkmcnt(4)\n\t"
      "v_exp_f32_e32 v200, v200\n\t"
      "v_mfma_f32_16x16x32_bf16 v[24:27], v[152:155], v[216:219], v[24:27]\n\t"
      "v_exp_f32_e32 v201, v201\n\t"
      "v_mfma_f32_16x16x32_bf16 v[28:31], v[156:159], v[216:219], v[28:31]\n\t"
      "s_cmp_eq_u32 s30, 2\n\t"
      "s_cbranch_scc1 L_nod_A3_%=_w\n\t"
      "s_waitcnt vmcnt(0)\n\t"
      "L_nod_A3_%=_w:\n\t"
      "s_waitcnt vmcnt(4) lgkmcnt(0)\n\t"
      "s_barrier\n\t"
      "v_mfma_f32_16x16x32_bf16 v[8:11], v[160:163], v[216:219], v[8:11]\n\t"
      "s_sub_u32 s30, s30, 1\n\t"
      "v_exp_f32_e32 v206, v206\n\t"
      "v_mfma_f32_16x16x32_bf16 v[12:15], v[164:167], v[216:219], v[12:15]\n\t"
      "v_cvt_pk_bf16_f32 v217, v194, v195\n\t"
      "v_mfma_f32_16x16x32_bf16 v[232:235], v[240:243], v[220:223], v[232:235]\n\t"
      "ds_read_b128 v[168:171], v248 offset:1024\n\t"
      "v_cvt_pk_bf16_f32 v216, v192, v193\n\t"
      "v_mfma_f32_16x16x32_bf16 v[32:35], v[176:179], v[220:223], v[32:35]\n\t"
      "v_cvt_pk_bf16_f32 v219, v198, v199\n\t"
      "v_mfma_f32_16x16x32_bf16 v[36:39], v[180:183], v[220:223], v[36:39]\n\t"
      "ds_read_b128 v[172:175], v248 offset:1536\n\t"
      "v_cvt_pk_bf16_f32 v218, v196, v197\n\t"
      "v_mfma_f32_16x16x32_bf16 v[48:51], v[144:147], v[220:223], v[48:51]\n\t"
      "v_mfma_f32_16x16x32_bf16 v[52:55], v[148:151], v[220:223], v[52:55]\n\t"
      "v_exp_f32_e32 v207, v207\n\t"
      "v_mfma_f32_16x16x32_bf16 v[56:59], v[152:155], v[220:223], v[56:59]\n\t"
      "v_mfma_f32_16x16x32_bf16 v[60:63], v[156:159], v[220:223], v[60:63]\n\t"
      "v_exp_f32_e32 v204, v204\n\t"
      "v_mfma_f32_16x16x32_bf16 v[40:43], v[160:163], v[220:223], v[40:43]\n\t"
      "v_exp_f32_e32 v205, v205\n\t"
      "v_mfma_f32_16x16x32_bf16 v[44:47], v[164:167], v[220:223], v[44:47]\n\t"
      "v_cvt_pk_bf16_f32 v221, v202, v203\n\t"
      "v_cvt_pk_bf16_f32 v220, v200, v201\n\t"
      "v_mfma_f32_16x16x32_bf16 v[72:75], v[160:163], v[224:227], v[72:75]\n\t"
      "ds_read_b128 v[160:163], v248 offset:0\n\t"
      "v_cvt_pk_bf16_f32 v223, v206, v207\n\t"
      "v_mfma_f32_16x16x32_bf16 v[76:79], v[164:167], v[224:227], v[76:79]\n\t"
      "ds_read_b128 v[164:167], v248 offset:512\n\t"
      "v_cvt_pk_bf16_f32 v222, v204, v205\n\t"
      "v_mfma_f32_16x16x32_bf16 v[80:83], v[144:147], v[224:227], v[80:83]\n\t"
      "ds_read_b128 v[144:147], v248 offset:4096\n\t"
      "v_mfma_f32_16x16x32_bf16 v[84:87], v[148:151], v[224:227], v[84:87]\n\t"
      "v_exp_f32_e32 v215, v215\n\t"
      "v_mfma_f32_16x16x32_bf16 v[88:91], v[152:155], v[224:227], v[88:91]\n\t"
      "v_exp_f32_e32 v213, v213\n\t"
      "v_mfma_f32_16x16x32_bf16 v[92:95], v[156:159], v[224:227], v[92:95]\n\t"
      "ds_read_b128 v[148:151], v248 offset:4608\n\t"
      "v_mfma_f32_16x16x32_bf16 v[64:67], v[176:179], v[224:227], v[64:67]\n\t"
      "ds_read_b128 v[152:155], v248 offset:5120\n\t"
      "ds_read_b128 v[156:159], v248 offset:5632\n\t"
      "v_mfma_f32_16x16x32_bf16 v[68:71], v[180:183], v[224:227], v[68:71]\n\t"
      "s_cmp_ge_u32 s30, 1\n\t"
      "v_exp_f32_e32 v212, v212\n\t"
      "v_mfma_f32_16x16x32_bf16 v[236:239], v[240:243], v[224:227], v[236:239]\n\t"
      "s_waitcnt lgkmcnt(4)\n\t"
      "s_cbranch_scc1 L_nod_A3_%=\n\t"
      "L_dr_A3_%=:\n\t"
      "v_mfma_f32_16x16x32_bf16 v[228:231], v[240:243], v[216:219], v[228:231]\n\t"
      "ds_read_b64_tr_b16 v[176:177], v249 offset:0\n\t"
      "ds_read_b64_tr_b16 v[178:179], v249 offset:4096\n\t"
      "v_mfma_f32_16x16x32_bf16 v[232:235], v[240:243], v[220:223], v[232:235]\n\t"
      "ds_read_b64_tr_b16 v[180:181], v250 offset:0\n\t"
      "ds_read_b64_tr_b16 v[182:183], v250 offset:4096\n\t"
      "ds_read_b64_tr_b16 v[160:161], v249 offset:512\n\t"
      "ds_read_b64_tr_b16 v[162:163], v249 offset:4608\n\t"
      "ds_read_b64_tr_b16 v[164:165], v250 offset:512\n\t"
      "ds_read_b64_tr_b16 v[166:167], v250 offset:4608\n\t"
      "ds_read_b64_tr_b16 v[144:145], v249 offset:1024\n\t"
      "ds_read_b64_tr_b16 v[146:147], v249 offset:5120\n\t"
      "ds_read_b64_tr_b16 v[148:149], v250 offset:1024\n\t"
      "v_exp_f32_e32 v214, v214\n\t"
      "v_exp_f32_e32 v209, v209\n\t"
      "v_exp_f32_e32 v208, v208\n\t"
      "v_exp_f32_e32 v210, v210\n\t"
      "v_exp_f32_e32 v211, v211\n\t"
      "v_cvt_pk_bf16_f32 v226, v212, v213\n\t"
      "v_cvt_pk_bf16_f32 v227, v214, v215\n\t"
      "v_cvt_pk_bf16_f32 v224, v208, v209\n\t"
      "v_cvt_pk_bf16_f32 v225, v210, v211\n\t"
      "ds_read_b64_tr_b16 v[150:151], v250 offset:5120\n\t"
      "s_waitcnt lgkmcnt(8)\n\t"
      "v_mfma_f32_16x16x32_bf16 v[0:3], v[176:179], v[216:219], v[0:3]\n\t"
      "ds_read_b64_tr_b16 v[152:153], v249 offset:1536\n\t"
      "ds_read_b64_tr_b16 v[154:155], v249 offset:5632\n\t"
      "v_mfma_f32_16x16x32_bf16 v[4:7], v[180:183], v[216:219], v[4:7]\n\t"
      "ds_read_b64_tr_b16 v[156:157], v250 offset:1536\n\t"
      "ds_read_b64_tr_b16 v[158:159], v250 offset:5632\n\t"
      "s_waitcnt lgkmcnt(8)\n\t"
      "v_mfma_f32_16x16x32_bf16 v[8:11], v[160:163], v[216:219], v[8:11]\n\t"
      "s_lshr_b32 s37, s42, 14\n\t"
      "v_rcp_f32_e32 v230, v228\n\t"
      "v_mfma_f32_16x16x32_bf16 v[12:15], v[164:167], v[216:219], v[12:15]\n\t"
      "s_add_u32 s37, s37, s26\n\t"
      "v_mul_f32_e32 v0, v230, v0\n\t"
      "v_mul_f32_e32 v1, v230, v1\n\t"
      "v_mfma_f32_16x16x32_bf16 v[236:239], v[240:243], v[224:227], v[236:239]\n\t"
      "s_add_u32 s37, s37, 1\n\t"
      "v_mul_f32_e32 v2, v230, v2\n\t"
      "v_mul_f32_e32 v3, v230, v3\n\t"
      "s_waitcnt lgkmcnt(4)\n\t"
      "v_mfma_f32_16x16x32_bf16 v[16:19], v[144:147], v[216:219], v[16:19]\n\t"
      "s_sub_u32 s38, s37, 5\n\t"
      "s_cselect_b32 s37, s37, s38\n\t"
      "v_mul_f32_e32 v4, v230, v4\n\t"
      "v_mul_f32_e32 v5, v230, v5\n\t"
      "v_mfma_f32_16x16x32_bf16 v[32:35], v[176:179], v[220:223], v[32:35]\n\t"
      "s_lshl_b32 s42, s37, 14\n\t"
      "v_cvt_pk_bf16_f32 v0, v0, v1\n\t"
      "v_cvt_pk_bf16_f32 v1, v2, v3\n\t"
      "v_mfma_f32_16x16x32_bf16 v[20:23], v[148:151], v[216:219], v[20:23]\n\t"
      "v_mul_f32_e32 v6, v230, v6\n\t"
      "v_mul_f32_e32 v7, v230, v7\n\t"
      "s_waitcnt lgkmcnt(0)\n\t"
      "v_mfma_f32_16x16x32_bf16 v[24:27], v[152:155], v[216:219], v[24:27]\n\t"
      "v_mul_f32_e32 v8, v230, v8\n\t"
      "v_mul_f32_e32 v9, v230, v9\n\t"
      "v_mfma_f32_16x16x32_bf16 v[28:31], v[156:159], v[216:219], v[28:31]\n\t"
      "v_cvt_pk_bf16_f32 v4, v4, v5\n\t"
      "v_cvt_pk_bf16_f32 v5, v6, v7\n\t"
      "v_mfma_f32_16x16x32_bf16 v[36:39], v[180:183], v[220:223], v[36:39]\n\t"
      "v_mul_f32_e32 v10, v230, v10\n\t"
      "v_mul_f32_e32 v11, v230, v11\n\t"
      "v_mfma_f32_16x16x32_bf16 v[40:43], v[160:163], v[220:223], v[40:43]\n\t"
      "v_mul_f32_e32 v12, v230, v12\n\t"
      "v_mul_f32_e32 v13, v230, v13\n\t"
      "v_mfma_f32_16x16x32_bf16 v[44:47], v[164:167], v[220:223], v[44:47]\n\t"
      "v_cvt_pk_bf16_f32 v8, v8, v9\n\t"
      "v_cvt_pk_bf16_f32 v9, v10, v11\n\t"
      "v_mfma_f32_16x16x32_bf16 v[48:51], v[144:147], v[220:223], v[48:51]\n\t"
      "v_mul_f32_e32 v14, v230, v14\n\t"
      "v_mul_f32_e32 v15, v230, v15\n\t"
      "v_mfma_f32_16x16x32_bf16 v[52:55], v[148:151], v[220:223], v[52:55]\n\t"
      "v_cvt_pk_bf16_f32 v12, v12, v13\n\t"
      "v_cvt_pk_bf16_f32 v13, v14, v15\n\t"
      "v_mfma_f32_16x16x32_bf16 v[56:59], v[152:155], v[220:223], v[56:59]\n\t"
      "v_add_u32_e32 v253, s42, v253\n\t"
      "v_add_u32_e32 v251, s42, v251\n\t"
      "v_mfma_f32_16x16x32_bf16 v[60:63], v[156:159], v[220:223], v[60:63]\n\t"
      "v_xor_b32_e32 v248, 32, v253\n\t"
      "v_xor_b32_e32 v254, 64, v253\n\t"
      "v_mfma_f32_16x16x32_bf16 v[64:67], v[176:179], v[224:227], v[64:67]\n\t"
      "v_xor_b32_e32 v143, 96, v253\n\t"
      "ds_write_b64 v253, v[0:1] offset:0\n\t"
      "ds_write_b64 v248, v[4:5] offset:0\n\t"
      "ds_write_b64 v254, v[8:9] offset:0\n\t"
      "ds_write_b64 v143, v[12:13] offset:0\n\t"
      "v_mfma_f32_16x16x32_bf16 v[68:71], v[180:183], v[224:227], v[68:71]\n\t"
      "ds_read_b128 v[96:99], v251 offset:0\n\t"
      "ds_read_b128 v[100:103], v251 offset:1024\n\t"
      "v_mfma_f32_16x16x32_bf16 v[72:75], v[160:163], v[224:227], v[72:75]\n\t"
      "v_rcp_f32_e32 v234, v232\n\t"
      "v_mfma_f32_16x16x32_bf16 v[76:79], v[164:167], v[224:227], v[76:79]\n\t"
      "v_rcp_f32_e32 v238, v236\n\t"
      "v_mfma_f32_16x16x32_bf16 v[80:83], v[144:147], v[224:227], v[80:83]\n\t"
      "v_add_f32_e32 v229, v228, v232\n\t"
      "v_add_f32_e32 v229, v229, v236\n\t"
      "v_add_f32_e32 v229, v229, v230\n\t"
      "v_add_f32_e32 v229, v229, v234\n\t"
      "v_add_f32_e32 v229, v229, v238\n\t"
      "v_cmp_nge_f32_e32 vcc, 0x4f800000, v229\n\t"
      "v_mfma_f32_16x16x32_bf16 v[84:87], v[148:151], v[224:227], v[84:87]\n\t"
      "s_mov_b32 s39, 1\n\t"
      "s_cbranch_vccnz L_end_%=\n\t"
      "s_mov_b32 s39, 0\n\t"
      "v_mfma_f32_16x16x32_bf16 v[88:91], v[152:155], v[224:227], v[88:91]\n\t"
      "s_waitcnt lgkmcnt(1)\n\t"
      "buffer_store_dwordx4 v[96:99], v252, s[20:23], s27 offen nt sc1\n\t"
      "s_add_u32 s38, s27, 0x8000\n\t"
      "v_mfma_f32_16x16x32_bf16 v[92:95], v[156:159], v[224:227], v[92:95]\n\t"
      "s_waitcnt lgkmcnt(0)\n\t"
      "v_mov_b32_e32 v104, v100\n\t"
      "v_mov_b32_e32 v105, v101\n\t"
      "buffer_store_dwordx4 v[102:105], v252, s[20:23], s38 offen nt sc1\n\t"
      "v_mul_f32_e32 v16, v230, v16\n\t"
      "v_mul_f32_e32 v17, v230, v17\n\t"
      "v_mul_f32_e32 v18, v230, v18\n\t"
      "v_mul_f32_e32 v19, v230, v19\n\t"
      "v_mul_f32_e32 v20, v230, v20\n\t"
      "v_mul_f32_e32 v21, v230, v21\n\t"
      "v_cvt_pk_bf16_f32 v16, v16, v17\n\t"
      "v_cvt_pk_bf16_f32 v17, v18, v19\n\t"
      "v_mul_f32_e32 v22, v230, v22\n\t"
      "v_mul_f32_e32 v23, v230, v23\n\t"
      "v_mul_f32_e32 v24, v230, v24\n\t"
      "v_mul_f32_e32 v25, v230, v25\n\t"
      "v_cvt_pk_bf16_f32 v20, v20, v21\n\t"
      "v_cvt_pk_bf16_f32 v21, v22, v23\n\t"
      "v_mul_f32_e32 v26, v230, v26\n\t"
      "v_mul_f32_e32 v27, v230, v27\n\t"
      "v_mul_f32_e32 v28, v230, v28\n\t"
      "v_mul_f32_e32 v29, v230, v29\n\t"
      "v_cvt_pk_bf16_f32 v24, v24, v25\n\t"
      "v_cvt_pk_bf16_f32 v25, v26, v27\n\t"
      "v_mul_f32_e32 v30, v230, v30\n\t"
      "v_mul_f32_e32 v31, v230, v31\n\t"
      "v_cvt_pk_bf16_f32 v28, v28, v29\n\t"
      "v_cvt_pk_bf16_f32 v29, v30, v31\n\t"
      "ds_write_b64 v253, v[16:17] offset:2048\n\t"
      "ds_write_b64 v248, v[20:21] offset:2048\n\t"
      "ds_write_b64 v254, v[24:25] offset:2048\n\t"
      "ds_write_b64 v143, v[28:29] offset:2048\n\t"
      "ds_read_b128 v[106:109], v251 offset:2048\n\t"
      "ds_read_b128 v[110:113], v251 offset:3072\n\t"
      "v_mul_f32_e32 v32, v234, v32\n\t"
      "v_mul_f32_e32 v33, v234, v33\n\t"
      "v_mul_f32_e32 v34, v234, v34\n\t"
      "v_mul_f32_e32 v35, v234, v35\n\t"
      "v_mul_f32_e32 v36, v234, v36\n\t"
      "v_mul_f32_e32 v37, v234, v37\n\t"
      "v_cvt_pk_bf16_f32 v32, v32, v33\n\t"
      "v_cvt_pk_bf16_f32 v33, v34, v35\n\t"
      "v_mul_f32_e32 v38, v234, v38\n\t"
      "v_mul_f32_e32 v39, v234, v39\n\t"
      "v_mul_f32_e32 v40, v234, v40\n\t"
      "v_mul_f32_e32 v41, v234, v41\n\t"
      "v_cvt_pk_bf16_f32 v36, v36, v37\n\t"
      "v_cvt_pk_bf16_f32 v37, v38, v39\n\t"
      "v_mul_f32_e32 v42, v234, v42\n\t"
      "v_mul_f32_e32 v43, v234, v43\n\t"
      "v_mul_f32_e32 v44, v234, v44\n\t"
      "v_mul_f32_e32 v45, v234, v45\n\t"
      "v_cvt_pk_bf16_f32 v40, v40, v41\n\t"
      "v_cvt_pk_bf16_f32 v41, v42, v43\n\t"
      "v_mul_f32_e32 v46, v234, v46\n\t"
      "v_mul_f32_e32 v47, v234, v47\n\t"
      "s_waitcnt lgkmcnt(1)\n\t"
      "buffer_store_dwordx4 v[106:109], v252, s[20:23], s27 offen offset:128 nt sc1\n\t"
      "s_waitcnt lgkmcnt(0)\n\t"
      "v_mov_b32_e32 v114, v110\n\t"
      "v_mov_b32_e32 v115, v111\n\t"
      "buffer_store_dwordx4 v[112:115], v252, s[20:23], s38 offen offset:128 nt sc1\n\t"
      "s_add_u32 s27, s27, 0x10000\n\t"
      "v_cvt_pk_bf16_f32 v44, v44, v45\n\t"
      "v_cvt_pk_bf16_f32 v45, v46, v47\n\t"
      "ds_write_b64 v253, v[32:33] offset:4096\n\t"
      "ds_write_b64 v248, v[36:37] offset:4096\n\t"
      "ds_write_b64 v254, v[40:41] offset:4096\n\t"
      "ds_write_b64 v143, v[44:45] offset:4096\n\t"
      "ds_read_b128 v[116:119], v251 offset:4096\n\t"
      "ds_read_b128 v[120:123], v251 offset:5120\n\t"
      "v_mul_f32_e32 v48, v234, v48\n\t"
      "v_mul_f32_e32 v49, v234, v49\n\t"
      "v_mul_f32_e32 v50, v234, v50\n\t"
      "v_mul_f32_e32 v51, v234, v51\n\t"
      "v_mul_f32_e32 v52, v234, v52\n\t"
      "v_mul_f32_e32 v53, v234, v53\n\t"
      "v_cvt_pk_bf16_f32 v48, v48, v49\n\t"
      "v_cvt_pk_bf16_f32 v49, v50, v51\n\t"
      "v_mul_f32_e32 v54, v234, v54\n\t"
      "v_mul_f32_e32 v55, v234, v55\n\t"
      "v_mul_f32_e32 v56, v234, v56\n\t"
      "v_mul_f32_e32 v57, v234, v57\n\t"
      "v_cvt_pk_bf16_f32 v52, v52, v53\n\t"
      "v_cvt_pk_bf16_f32 v53, v54, v55\n\t"
      "v_mul_f32_e32 v58, v234, v58\n\t"
      "v_mul_f32_e32 v59, v234, v59\n\t"
      "v_mul_f32_e32 v60, v234, v60\n\t"
      "v_mul_f32_e32 v61, v234, v61\n\t"
      "v_cvt_pk_bf16_f32 v56, v56, v57\n\t"
      "v_cvt_pk_bf16_f32 v57, v58, v59\n\t"
      "v_mul_f32_e32 v62, v234, v62\n\t"
      "v_mul_f32_e32 v63, v234, v63\n\t"
      "s_waitcnt lgkmcnt(1)\n\t"
      "buffer_store_dwordx4 v[116:119], v252, s[20:23], s27 offen nt sc1\n\t"
      "s_add_u32 s38, s27, 0x8000\n\t"
      "s_waitcnt lgkmcnt(0)\n\t"
      "v_mov_b32_e32 v124, v120\n\t"
      "v_mov_b32_e32 v125, v121\n\t"
      "buffer_store_dwordx4 v[122:125], v252, s[20:23], s38 offen nt sc1\n\t"
      "v_cvt_pk_bf16_f32 v60, v60, v61\n\t"
      "v_cvt_pk_bf16_f32 v61, v62, v63\n\t"
      "ds_write_b64 v253, v[48:49] offset:6144\n\t"
      "ds_write_b64 v248, v[52:53] offset:6144\n\t"
      "ds_write_b64 v254, v[56:57] offset:6144\n\t"
      "ds_write_b64 v143, v[60:61] offset:6144\n\t"
      "ds_read_b128 v[126:129], v251 offset:6144\n\t"
      "ds_read_b128 v[130:133], v251 offset:7168\n\t"
      "v_mul_f32_e32 v64, v238, v64\n\t"
      "v_mul_f32_e32 v65, v238, v65\n\t"
      "v_mul_f32_e32 v66, v238, v66\n\t"
      "v_mul_f32_e32 v67, v238, v67\n\t"
      "v_mul_f32_e32 v68, v238, v68\n\t"
      "v_mul_f32_e32 v69, v238, v69\n\t"
      "v_cvt_pk_bf16_f32 v64, v64, v65\n\t"
      "v_cvt_pk_bf16_f32 v65, v66, v67\n\t"
      "v_mul_f32_e32 v70, v238, v70\n\t"
      "v_mul_f32_e32 v71, v238, v71\n\t"
      "v_mul_f32_e32 v72, v238, v72\n\t"
      "v_mul_f32_e32 v73, v238, v73\n\t"
      "v_cvt_pk_bf16_f32 v68, v68, v69\n\t"
      "v_cvt_pk_bf16_f32 v69, v70, v71\n\t"
      "v_mul_f32_e32 v74, v238, v74\n\t"
      "v_mul_f32_e32 v75, v238, v75\n\t"
      "v_mul_f32_e32 v76, v238, v76\n\t"
      "v_mul_f32_e32 v77, v238, v77\n\t"
      "v_cvt_pk_bf16_f32 v72, v72, v73\n\t"
      "v_cvt_pk_bf16_f32 v73, v74, v75\n\t"
      "v_mul_f32_e32 v78, v238, v78\n\t"
      "v_mul_f32_e32 v79, v238, v79\n\t"
      "s_waitcnt lgkmcnt(1)\n\t"
      "buffer_store_dwordx4 v[126:129], v252, s[20:23], s27 offen offset:128 nt sc1\n\t"
      "s_waitcnt lgkmcnt(0)\n\t"
      "v_mov_b32_e32 v134, v130\n\t"
      "v_mov_b32_e32 v135, v131\n\t"
      "buffer_store_dwordx4 v[132:135], v252, s[20:23], s38 offen offset:128 nt sc1\n\t"
      "s_add_u32 s27, s27, 0x10000\n\t"
      "v_cvt_pk_bf16_f32 v76, v76, v77\n\t"
      "v_cvt_pk_bf16_f32 v77, v78, v79\n\t"
      "ds_write_b64 v253, v[64:65] offset:8192\n\t"
      "ds_write_b64 v248, v[68:69] offset:8192\n\t"
      "ds_write_b64 v254, v[72:73] offset:8192\n\t"
      "ds_write_b64 v143, v[76:77] offset:8192\n\t"
      "ds_read_b128 v[144:147], v251 offset:8192\n\t"
      "ds_read_b128 v[148:151], v251 offset:9216\n\t"
      "v_mul_f32_e32 v80, v238, v80\n\t"
      "v_mul_f32_e32 v81, v238, v81\n\t"
      "v_mul_f32_e32 v82, v238, v82\n\t"
      "v_mul_f32_e32 v83, v238, v83\n\t"
      "v_mul_f32_e32 v84, v238, v84\n\t"
      "v_mul_f32_e32 v85, v238, v85\n\t"
      "v_cvt_pk_bf16_f32 v80, v80, v81\n\t"
      "v_cvt_pk_bf16_f32 v81, v82, v83\n\t"
      "v_mul_f32_e32 v86, v238, v86\n\t"
      "v_mul_f32_e32 v87, v238, v87\n\t"
      "v_mul_f32_e32 v88, v238, v88\n\t"
      "v_mul_f32_e32 v89, v238, v89\n\t"
      "v_cvt_pk_bf16_f32 v84, v84, v85\n\t"
      "v_cvt_pk_bf16_f32 v85, v86, v87\n\t"
      "v_mul_f32_e32 v90, v238, v90\n\t"
      "v_mul_f32_e32 v91, v238, v91\n\t"
      "v_mul_f32_e32 v92, v238, v92\n\t"
      "v_mul_f32_e32 v93, v238, v93\n\t"
      "v_cvt_pk_bf16_f32 v88, v88, v89\n\t"
      "v_cvt_pk_bf16_f32 v89, v90, v91\n\t"
      "v_mul_f32_e32 v94, v238, v94\n\t"
      "v_mul_f32_e32 v95, v238, v95\n\t"
      "s_waitcnt lgkmcnt(1)\n\t"
      "buffer_store_dwordx4 v[144:147], v252, s[20:23], s27 offen nt sc1\n\t"
      "s_add_u32 s38, s27, 0x8000\n\t"
      "s_waitcnt lgkmcnt(0)\n\t"
      "v_mov_b32_e32 v152, v148\n\t"
      "v_mov_b32_e32 v153, v149\n\t"
      "buffer_store_dwordx4 v[150:153], v252, s[20:23], s38 offen nt sc1\n\t"
      "v_cvt_pk_bf16_f32 v92, v92, v93\n\t"
      "v_cvt_pk_bf16_f32 v93, v94, v95\n\t"
      "ds_write_b64 v253, v[80:81] offset:10240\n\t"
      "ds_write_b64 v248, v[84:85] offset:10240\n\t"
      "ds_write_b64 v254, v[88:89] offset:10240\n\t"
      "ds_write_b64 v143, v[92:93] offset:10240\n\t"
      "ds_read_b128 v[154:157], v251 offset:10240\n\t"
      "ds_read_b128 v[158:161], v251 offset:11264\n\t"
      "s_waitcnt lgkmcnt(1)\n\t"
      "buffer_store_dwordx4 v[154:157], v252, s[20:23], s27 offen offset:128 nt sc1\n\t"
      "s_waitcnt lgkmcnt(0)\n\t"
      "v_mov_b32_e32 v162, v158\n\t"
      "v_mov_b32_e32 v163, v159\n\t"
      "buffer_store_dwordx4 v[160:163], v252, s[20:23], s38 offen offset:128 nt sc1\n\t"
      "s_add_u32 s27, s27, 0x10000\n\t"
      "s_add_u32 s37, s7, 16\n\t"
      "s_cmp_lt_u32 s37, s54\n\t"
      "s_cbranch_scc1 L_end_%=\n\t"
      "s_cmp_lg_u32 s43, 0\n\t"
      "s_cbranch_scc1 L_end_%=\n\t"
      "s_endpgm\n\t"
      "L_A2_%=:\n\t"
      "s_waitcnt vmcnt(8)\n\t"
      "s_barrier\n\t"
      "ds_read_b128 v[160:163], v248 offset:0\n\t"
      "ds_read_b128 v[164:167], v248 offset:512\n\t"
      "ds_read_b128 v[168:171], v248 offset:1024\n\t"
      "ds_read_b128 v[172:175], v248 offset:1536\n\t"
      "ds_read_b128 v[144:147], v248 offset:4096\n\t"
      "ds_read_b128 v[148:151], v248 offset:4608\n\t"
      "ds_read_b128 v[152:155], v248 offset:5120\n\t"
      "ds_read_b128 v[156:159], v248 offset:5632\n\t"
      "s_add_u32 m0, s42, 16384\n\t"
      "s_nop 0\n\t"
      "buffer_load_dwordx4 v251, s[12:15], s31 offen lds\n\t"
      "s_addk_i32 m0, 896\n\t"
      "s_nop 0\n\t"
      "buffer_load_dwordx4 v251, s[12:15], s31 offen offset:128 lds\n\t"
      "s_addk_i32 m0, 7296\n\t"
      "s_nop 0\n\t"
      "buffer_load_dwordx4 v252, s[16:19], s41 offen lds\n\t"
      "s_addk_i32 m0, 896\n\t"
      "s_nop 0\n\t"
      "buffer_load_dwordx4 v252, s[16:19], s41 offen offset:128 lds\n\t"
      "s_mov_b32 s18, s41\n\t"
      "s_mov_b32 s41, s31\n\t"
      "s_add_u32 s31, s31, 0x20000\n\t"
      "s_waitcnt vmcnt(8)\n\t"
      "ds_read_b128 v[96:99], v254 offset:0\n\t"
      "ds_read_b128 v[100:103], v254 offset:512\n\t"
      "ds_read_b128 v[104:107], v254 offset:1024\n\t"
      "ds_read_b128 v[108:111], v254 offset:1536\n\t"
      "s_waitcnt lgkmcnt(3)\n\t"
      "v_lshlrev_b32_e32 v192, 16, v96\n\t"
      "v_and_b32_e32 v193, s60, v96\n\t"
      "v_lshlrev_b32_e32 v194, 16, v97\n\t"
      "v_and_b32_e32 v195, s60, v97\n\t"
      "v_lshlrev_b32_e32 v196, 16, v98\n\t"
      "v_and_b32_e32 v197, s60, v98\n\t"
      "v_lshlrev_b32_e32 v198, 16, v99\n\t"
      "v_and_b32_e32 v199, s60, v99\n\t"
      "v_pk_mul_f32 v[192:193], v[192:193], s[36:37] op_sel_hi:[1,0]\n\t"
      "v_pk_mul_f32 v[194:195], v[194:195], s[36:37] op_sel_hi:[1,0]\n\t"
      "v_pk_mul_f32 v[196:197], v[196:197], s[36:37] op_sel_hi:[1,0]\n\t"
      "v_pk_mul_f32 v[198:199], v[198:199], s[36:37] op_sel_hi:[1,0]\n\t"
      "v_cvt_pk_bf16_f32 v96, v192, v193\n\t"
      "v_cvt_pk_bf16_f32 v97, v194, v195\n\t"
      "v_cvt_pk_bf16_f32 v98, v196, v197\n\t"
      "v_cvt_pk_bf16_f32 v99, v198, v199\n\t"
      "s_waitcnt lgkmcnt(2)\n\t"
      "v_lshlrev_b32_e32 v192, 16, v100\n\t"
      "v_and_b32_e32 v193, s60, v100\n\t"
      "v_lshlrev_b32_e32 v194, 16, v101\n\t"
      "v_and_b32_e32 v195, s60, v101\n\t"
      "v_lshlrev_b32_e32 v196, 16, v102\n\t"
      "v_and_b32_e32 v197, s60, v102\n\t"
      "v_lshlrev_b32_e32 v198, 16, v103\n\t"
      "v_and_b32_e32 v199, s60, v103\n\t"
      "v_pk_mul_f32 v[192:193], v[192:193], s[36:37] op_sel_hi:[1,0]\n\t"
      "v_pk_mul_f32 v[194:195], v[194:195], s[36:37] op_sel_hi:[1,0]\n\t"
      "v_pk_mul_f32 v[196:197], v[196:197], s[36:37] op_sel_hi:[1,0]\n\t"
      "v_pk_mul_f32 v[198:199], v[198:199], s[36:37] op_sel_hi:[1,0]\n\t"
      "v_cvt_pk_bf16_f32 v100, v192, v193\n\t"
      "v_cvt_pk_bf16_f32 v101, v194, v195\n\t"
      "v_cvt_pk_bf16_f32 v102, v196, v197\n\t"
      "v_cvt_pk_bf16_f32 v103, v198, v199\n\t"
      "s_waitcnt lgkmcnt(1)\n\t"
      "v_lshlrev_b32_e32 v192, 16, v104\n\t"
      "v_and_b32_e32 v193, s60, v104\n\t"
      "v_lshlrev_b32_e32 v194, 16, v105\n\t"
      "v_and_b32_e32 v195, s60, v105\n\t"
      "v_lshlrev_b32_e32 v196, 16, v106\n\t"
      "v_and_b32_e32 v197, s60, v106\n\t"
      "v_lshlrev_b32_e32 v198, 16, v107\n\t"
      "v_and_b32_e32 v199, s60, v107\n\t"
      "v_pk_mul_f32 v[192:193], v[192:193], s[36:37] op_sel_hi:[1,0]\n\t"
      "v_pk_mul_f32 v[194:195], v[194:195], s[36:37] op_sel_hi:[1,0]\n\t"
      "v_pk_mul_f32 v[196:197], v[196:197], s[36:37] op_sel_hi:[1,0]\n\t"
      "v_pk_mul_f32 v[198:199], v[198:199], s[36:37] op_sel_hi:[1,0]\n\t"
      "v_cvt_pk_bf16_f32 v104, v192, v193\n\t"
      "v_cvt_pk_bf16_f32 v105, v194, v195\n\t"
      "v_cvt_pk_bf16_f32 v106, v196, v197\n\t"
      "v_cvt_pk_bf16_f32 v107, v198, v199\n\t"
      "s_waitcnt lgkmcnt(0)\n\t"
      "v_lshlrev_b32_e32 v192, 16, v108\n\t"
      "v_and_b32_e32 v193, s60, v108\n\t"
      "v_lshlrev_b32_e32 v194, 16, v109\n\t"
      "v_and_b32_e32 v195, s60, v109\n\t"
      "v_lshlrev_b32_e32 v196, 16, v110\n\t"
      "v_and_b32_e32 v197, s60, v110\n\t"
      "v_lshlrev_b32_e32 v198, 16, v111\n\t"
      "v_and_b32_e32 v199, s60, v111\n\t"
      "v_pk_mul_f32 v[192:193], v[192:193], s[36:37] op_sel_hi:[1,0]\n\t"
      "v_pk_mul_f32 v[194:195], v[194:195], s[36:37] op_sel_hi:[1,0]\n\t"
      "v_pk_mul_f32 v[196:197], v[196:197], s[36:37] op_sel_hi:[1,0]\n\t"
      "v_pk_mul_f32 v[198:199], v[198:199], s[36:37] op_sel_hi:[1,0]\n\t"
      "v_cvt_pk_bf16_f32 v108, v192, v193\n\t"
      "v_cvt_pk_bf16_f32 v109, v194, v195\n\t"
      "v_cvt_pk_bf16_f32 v110, v196, v197\n\t"
      "v_cvt_pk_bf16_f32 v111, v198, v199\n\t"
      "v_mfma_f32_16x16x32_bf16 v[192:195], v[160:163], v[96:99], v[176:179]\n\t"
      "v_mfma_f32_16x16x32_bf16 v[192:195], v[164:167], v[100:103], v[192:195]\n\t"
      "v_mfma_f32_16x16x32_bf16 v[192:195], v[168:171], v[104:107], v[192:195]\n\t"
      "v_mfma_f32_16x16x32_bf16 v[192:195], v[172:175], v[108:111], v[192:195]\n\t"
      "v_mfma_f32_16x16x32_bf16 v[196:199], v[144:147], v[96:99], v[180:183]\n\t"
      "v_mfma_f32_16x16x32_bf16 v[196:199], v[148:151], v[100:103], v[196:199]\n\t"
      "v_mfma_f32_16x16x32_bf16 v[196:199], v[152:155], v[104:107], v[196:199]\n\t"
      "v_mfma_f32_16x16x32_bf16 v[196:199], v[156:159], v[108:111], v[196:199]\n\t"
      "s_nop 3\n\t"
      "v_max3_f32 v184, v192, v193, v194\n\t"
      "s_nop 2\n\t"
      "v_max3_f32 v253, v195, v196, v197\n\t"
      "v_max3_f32 v184, v184, v198, v199\n\t"
      "v_max_f32_e32 v184, v184, v253\n\t"
      "v_mov_b32_e32 v253, v184\n\t"
      "s_nop 1\n\t"
      "v_permlane16_swap_b32_e32 v184, v253\n\t"
      "s_nop 0\n\t"
      "v_max_f32_e32 v184, v184, v253\n\t"
      "v_mov_b32_e32 v253, v184\n\t"
      "s_nop 1\n\t"
      "v_permlane32_swap_b32_e32 v184, v253\n\t"
      "s_nop 0\n\t"
      "v_max_f32_e32 v184, v184, v253\n\t"
      "v_sub_f32_e32 v192, v192, v184\n\t"
      "v_sub_f32_e32 v193, v193, v184\n\t"
      "v_sub_f32_e32 v194, v194, v184\n\t"
      "v_sub_f32_e32 v195, v195, v184\n\t"
      "v_sub_f32_e32 v196, v196, v184\n\t"
      "v_sub_f32_e32 v197, v197, v184\n\t"
      "v_sub_f32_e32 v198, v198, v184\n\t"
      "v_sub_f32_e32 v199, v199, v184\n\t"
      "v_sub_f32_e32 v187, 0, v184\n\t"
      "v_sub_f32_e32 v186, 0, v184\n\t"
      "v_sub_f32_e32 v185, 0, v184\n\t"
      "v_sub_f32_e32 v184, 0, v184\n\t"
      "v_exp_f32_e32 v194, v194\n\t"
      "v_exp_f32_e32 v195, v195\n\t"
      "v_exp_f32_e32 v193, v193\n\t"
      "v_exp_f32_e32 v192, v192\n\t"
      "v_exp_f32_e32 v197, v197\n\t"
      "v_exp_f32_e32 v198, v198\n\t"
      "v_cvt_pk_bf16_f32 v217, v194, v195\n\t"
      "v_exp_f32_e32 v199, v199\n\t"
      "v_cvt_pk_bf16_f32 v216, v192, v193\n\t"
      "v_exp_f32_e32 v196, v196\n\t"
      "s_nop 0\n\t"
      "v_cvt_pk_bf16_f32 v218, v196, v197\n\t"
      "s_waitcnt vmcnt(4)\n\t"
      "ds_read_b128 v[112:115], v254 offset:16384\n\t"
      "ds_read_b128 v[116:119], v254 offset:16896\n\t"
      "ds_read_b128 v[120:123], v254 offset:17408\n\t"
      "ds_read_b128 v[124:127], v254 offset:17920\n\t"
      "s_add_u32 m0, s42, 32768\n\t"
      "s_nop 0\n\t"
      "buffer_load_dwordx4 v251, s[12:15], s31 offen lds\n\t"
      "s_addk_i32 m0, 896\n\t"
      "s_nop 0\n\t"
      "buffer_load_dwordx4 v251, s[12:15], s31 offen offset:128 lds\n\t"
      "s_addk_i32 m0, 7296\n\t"
      "s_nop 0\n\t"
      "buffer_load_dwordx4 v252, s[16:19], s41 offen lds\n\t"
      "s_addk_i32 m0, 896\n\t"
      "s_nop 0\n\t"
      "buffer_load_dwordx4 v252, s[16:19], s41 offen offset:128 lds\n\t"
      "s_mov_b32 s41, s31\n\t"
      "s_add_u32 s31, s31, 0x20000\n\t"
      "s_waitcnt lgkmcnt(3)\n\t"
      "v_lshlrev_b32_e32 v200, 16, v112\n\t"
      "v_and_b32_e32 v201, s60, v112\n\t"
      "v_lshlrev_b32_e32 v202, 16, v113\n\t"
      "v_and_b32_e32 v203, s60, v113\n\t"
      "v_lshlrev_b32_e32 v204, 16, v114\n\t"
      "v_and_b32_e32 v205, s60, v114\n\t"
      "v_lshlrev_b32_e32 v206, 16, v115\n\t"
      "v_and_b32_e32 v207, s60, v115\n\t"
      "v_pk_mul_f32 v[200:201], v[200:201], s[36:37] op_sel_hi:[1,0]\n\t"
      "v_pk_mul_f32 v[202:203], v[202:203], s[36:37] op_sel_hi:[1,0]\n\t"
      "v_pk_mul_f32 v[204:205], v[204:205], s[36:37] op_sel_hi:[1,0]\n\t"
      "v_pk_mul_f32 v[206:207], v[206:207], s[36:37] op_sel_hi:[1,0]\n\t"
      "v_cvt_pk_bf16_f32 v112, v200, v201\n\t"
      "v_cvt_pk_bf16_f32 v113, v202, v203\n\t"
      "v_cvt_pk_bf16_f32 v114, v204, v205\n\t"
      "v_cvt_pk_bf16_f32 v115, v206, v207\n\t"
      "s_waitcnt lgkmcnt(2)\n\t"
      "v_lshlrev_b32_e32 v200, 16, v116\n\t"
      "v_and_b32_e32 v201, s60, v116\n\t"
      "v_lshlrev_b32_e32 v202, 16, v117\n\t"
      "v_and_b32_e32 v203, s60, v117\n\t"
      "v_lshlrev_b32_e32 v204, 16, v118\n\t"
      "v_and_b32_e32 v205, s60, v118\n\t"
      "v_lshlrev_b32_e32 v206, 16, v119\n\t"
      "v_and_b32_e32 v207, s60, v119\n\t"
      "v_pk_mul_f32 v[200:201], v[200:201], s[36:37] op_sel_hi:[1,0]\n\t"
      "v_pk_mul_f32 v[202:203], v[202:203], s[36:37] op_sel_hi:[1,0]\n\t"
      "v_pk_mul_f32 v[204:205], v[204:205], s[36:37] op_sel_hi:[1,0]\n\t"
      "v_pk_mul_f32 v[206:207], v[206:207], s[36:37] op_sel_hi:[1,0]\n\t"
      "v_cvt_pk_bf16_f32 v116, v200, v201\n\t"
      "v_cvt_pk_bf16_f32 v117, v202, v203\n\t"
      "v_cvt_pk_bf16_f32 v118, v204, v205\n\t"
      "v_cvt_pk_bf16_f32 v119, v206, v207\n\t"
      "s_waitcnt lgkmcnt(1)\n\t"
      "v_lshlrev_b32_e32 v200, 16, v120\n\t"
      "v_and_b32_e32 v201, s60, v120\n\t"
      "v_lshlrev_b32_e32 v202, 16, v121\n\t"
      "v_and_b32_e32 v203, s60, v121\n\t"
      "v_lshlrev_b32_e32 v204, 16, v122\n\t"
      "v_and_b32_e32 v205, s60, v122\n\t"
      "v_lshlrev_b32_e32 v206, 16, v123\n\t"
      "v_and_b32_e32 v207, s60, v123\n\t"
      "v_pk_mul_f32 v[200:201], v[200:201], s[36:37] op_sel_hi:[1,0]\n\t"
      "v_pk_mul_f32 v[202:203], v[202:203], s[36:37] op_sel_hi:[1,0]\n\t"
      "v_pk_mul_f32 v[204:205], v[204:205], s[36:37] op_sel_hi:[1,0]\n\t"
      "v_pk_mul_f32 v[206:207], v[206:207], s[36:37] op_sel_hi:[1,0]\n\t"
      "v_cvt_pk_bf16_f32 v120, v200, v201\n\t"
      "v_cvt_pk_bf16_f32 v121, v202, v203\n\t"
      "v_cvt_pk_bf16_f32 v122, v204, v205\n\t"
      "v_cvt_pk_bf16_f32 v123, v206, v207\n\t"
      "s_waitcnt lgkmcnt(0)\n\t"
      "v_lshlrev_b32_e32 v200, 16, v124\n\t"
      "v_and_b32_e32 v201, s60, v124\n\t"
      "v_lshlrev_b32_e32 v202, 16, v125\n\t"
      "v_and_b32_e32 v203, s60, v125\n\t"
      "v_lshlrev_b32_e32 v204, 16, v126\n\t"
      "v_and_b32_e32 v205, s60, v126\n\t"
      "v_lshlrev_b32_e32 v206, 16, v127\n\t"
      "v_and_b32_e32 v207, s60, v127\n\t"
      "v_pk_mul_f32 v[200:201], v[200:201], s[36:37] op_sel_hi:[1,0]\n\t"
      "v_pk_mul_f32 v[202:203], v[202:203], s[36:37] op_sel_hi:[1,0]\n\t"
      "v_pk_mul_f32 v[204:205], v[204:205], s[36:37] op_sel_hi:[1,0]\n\t"
      "v_pk_mul_f32 v[206:207], v[206:207], s[36:37] op_sel_hi:[1,0]\n\t"
      "v_cvt_pk_bf16_f32 v124, v200, v201\n\t"
      "v_cvt_pk_bf16_f32 v125, v202, v203\n\t"
      "v_cvt_pk_bf16_f32 v126, v204, v205\n\t"
      "v_cvt_pk_bf16_f32 v127, v206, v207\n\t"
      "v_mfma_f32_16x16x32_bf16 v[200:203], v[160:163], v[112:115], v[176:179]\n\t"
      "v_mfma_f32_16x16x32_bf16 v[200:203], v[164:167], v[116:119], v[200:203]\n\t"
      "v_mfma_f32_16x16x32_bf16 v[200:203], v[168:171], v[120:123], v[200:203]\n\t"
      "v_mfma_f32_16x16x32_bf16 v[200:203], v[172:175], v[124:127], v[200:203]\n\t"
      "v_mfma_f32_16x16x32_bf16 v[204:207], v[144:147], v[112:115], v[180:183]\n\t"
      "v_mfma_f32_16x16x32_bf16 v[204:207], v[148:151], v[116:119], v[204:207]\n\t"
      "v_mfma_f32_16x16x32_bf16 v[204:207], v[152:155], v[120:123], v[204:207]\n\t"
      "v_mfma_f32_16x16x32_bf16 v[204:207], v[156:159], v[124:127], v[204:207]\n\t"
      "s_cmp_lt_u32 s42, s40\n\t"
      "s_cselect_b32 s34, s35, s58\n\t"
      "s_mov_b32 m0, s42\n\t"
      "s_add_u32 s42, s42, s34\n\t"
      "v_add_u32_e32 v248, s34, v248\n\t"
      "v_add_u32_e32 v249, s34, v249\n\t"
      "v_add_u32_e32 v250, s34, v250\n\t"
      "s_cmp_lg_u32 s30, 1\n\t"
      "s_cbranch_scc1 L_pw_A2_%=_0\n\t"
      "s_waitcnt vmcnt(0)\n\t"
      "L_pw_A2_%=_0:\n\t"
      "s_waitcnt vmcnt(4)\n\t"
      "s_barrier\n\t"
      "ds_read_b128 v[168:171], v248 offset:1024\n\t"
      "ds_read_b128 v[172:175], v248 offset:1536\n\t"
      "ds_read_b128 v[160:163], v248 offset:0\n\t"
      "ds_read_b128 v[164:167], v248 offset:512\n\t"
      "ds_read_b128 v[144:147], v248 offset:4096\n\t"
      "ds_read_b128 v[148:151], v248 offset:4608\n\t"
      "ds_read_b128 v[152:155], v248 offset:5120\n\t"
      "ds_read_b128 v[156:159], v248 offset:5632\n\t"
      "s_cmp_lt_u32 s30, 3\n\t"
      "s_cbranch_scc1 L_pnd_A2_%=\n\t"
      "s_nop 0\n\t"
      "buffer_load_dwordx4 v251, s[12:15], s31 offen lds\n\t"
      "s_addk_i32 m0, 896\n\t"
      "s_nop 0\n\t"
      "buffer_load_dwordx4 v251, s[12:15], s31 offen offset:128 lds\n\t"
      "s_addk_i32 m0, 7296\n\t"
      "s_nop 0\n\t"
      "buffer_load_dwordx4 v252, s[16:19], s41 offen lds\n\t"
      "s_addk_i32 m0, 896\n\t"
      "s_nop 0\n\t"
      "buffer_load_dwordx4 v252, s[16:19], s41 offen offset:128 lds\n\t"
      "s_mov_b32 s41, s31\n\t"
      "s_add_u32 s31, s31, 0x20000\n\t"
      "L_pnd_A2_%=:\n\t"
      "s_sub_i32 s25, s30, 4\n\t"
      "s_max_i32 s25, s25, 3\n\t"
      "v_max3_f32 v188, v200, v201, v202\n\t"
      "v_max3_f32 v253, v203, v204, v205\n\t"
      "v_max3_f32 v188, v188, v206, v207\n\t"
      "v_max_f32_e32 v188, v188, v253\n\t"
      "v_mov_b32_e32 v253, v188\n\t"
      "s_nop 1\n\t"
      "v_permlane16_swap_b32_e32 v188, v253\n\t"
      "s_nop 0\n\t"
      "v_max_f32_e32 v188, v188, v253\n\t"
      "v_mov_b32_e32 v253, v188\n\t"
      "s_nop 1\n\t"
      "v_permlane32_swap_b32_e32 v188, v253\n\t"
      "s_nop 0\n\t"
      "v_max_f32_e32 v188, v188, v253\n\t"
      "v_sub_f32_e32 v200, v200, v188\n\t"
      "v_sub_f32_e32 v201, v201, v188\n\t"
      "v_sub_f32_e32 v202, v202, v188\n\t"
      "v_sub_f32_e32 v203, v203, v188\n\t"
      "v_sub_f32_e32 v204, v204, v188\n\t"
      "v_sub_f32_e32 v205, v205, v188\n\t"
      "v_sub_f32_e32 v206, v206, v188\n\t"
      "v_sub_f32_e32 v207, v207, v188\n\t"
      "v_sub_f32_e32 v191, 0, v188\n\t"
      "v_sub_f32_e32 v190, 0, v188\n\t"
      "v_sub_f32_e32 v189, 0, v188\n\t"
      "v_sub_f32_e32 v188, 0, v188\n\t"
      "v_exp_f32_e32 v207, v207\n\t"
      "s_sub_u32 s30, s30, 1\n\t"
      "s_cmp_lt_u32 s30, 3\n\t"
      "s_waitcnt lgkmcnt(4)\n\t"
      "s_cbranch_scc1 L_s1_A2_%=\n\t"
      "L_dm0_A2_%=:\n\t"
      "v_mfma_f32_16x16x32_bf16 v[192:195], v[160:163], v[96:99], v[184:187]\n\t"
      "s_cmp_lt_u32 s42, s40\n\t"
      "ds_read_b64_tr_b16 v[176:177], v249 offset:0\n\t"
      "v_exp_f32_e32 v204, v204\n\t"
      "v_mfma_f32_16x16x32_bf16 v[192:195], v[164:167], v[100:103], v[192:195]\n\t"
      "s_cselect_b32 s34, s35, s58\n\t"
      "ds_read_b64_tr_b16 v[178:179], v249 offset:4096\n\t"
      "v_add_u32_e32 v248, s34, v248\n\t"
      "v_mfma_f32_16x16x32_bf16 v[192:195], v[168:171], v[104:107], v[192:195]\n\t"
      "s_mov_b32 m0, s42\n\t"
      "ds_read_b64_tr_b16 v[180:181], v250 offset:0\n\t"
      "v_exp_f32_e32 v205, v205\n\t"
      "v_mfma_f32_16x16x32_bf16 v[192:195], v[172:175], v[108:111], v[192:195]\n\t"
      "s_waitcnt lgkmcnt(3)\n\t"
      "ds_read_b64_tr_b16 v[182:183], v250 offset:4096\n\t"
      "v_cvt_pk_bf16_f32 v219, v198, v199\n\t"
      "v_mfma_f32_16x16x32_bf16 v[196:199], v[144:147], v[96:99], v[184:187]\n\t"
      "s_add_u32 s42, s42, s34\n\t"
      "v_exp_f32_e32 v202, v202\n\t"
      "v_mfma_f32_16x16x32_bf16 v[196:199], v[148:151], v[100:103], v[196:199]\n\t"
      "v_exp_f32_e32 v201, v201\n\t"
      "v_mfma_f32_16x16x32_bf16 v[196:199], v[152:155], v[104:107], v[196:199]\n\t"
      "v_exp_f32_e32 v206, v206\n\t"
      "v_mfma_f32_16x16x32_bf16 v[196:199], v[156:159], v[108:111], v[196:199]\n\t"
      "v_cvt_pk_bf16_f32 v222, v204, v205\n\t"
      "v_cvt_pk_bf16_f32 v223, v206, v207\n\t"
      "v_mfma_f32_16x16x32_bf16 v[204:207], v[144:147], v[112:115], v[188:191]\n\t"
      "ds_read_b64_tr_b16 v[144:145], v249 offset:1024\n\t"
      "v_exp_f32_e32 v200, v200\n\t"
      "v_mfma_f32_16x16x32_bf16 v[204:207], v[148:151], v[116:119], v[204:207]\n\t"
      "ds_read_b64_tr_b16 v[146:147], v249 offset:5120\n\t"
      "v_cvt_pk_bf16_f32 v220, v200, v201\n\t"
      "v_mfma_f32_16x16x32_bf16 v[204:207], v[152:155], v[120:123], v[204:207]\n\t"
      "ds_read_b64_tr_b16 v[148:149], v250 offset:1024\n\t"
      "v_exp_f32_e32 v203, v203\n\t"
      "v_mfma_f32_16x16x32_bf16 v[204:207], v[156:159], v[124:127], v[204:207]\n\t"
      "ds_read_b64_tr_b16 v[150:151], v250 offset:5120\n\t"
      "v_cvt_pk_bf16_f32 v221, v202, v203\n\t"
      "v_mfma_f32_16x16x32_bf16 v[200:203], v[160:163], v[112:115], v[188:191]\n\t"
      "ds_read_b64_tr_b16 v[152:153], v249 offset:1536\n\t"
      "v_mfma_f32_16x16x32_bf16 v[200:203], v[164:167], v[116:119], v[200:203]\n\t"
      "ds_read_b64_tr_b16 v[154:155], v249 offset:5632\n\t"
      "v_mfma_f32_16x16x32_bf16 v[200:203], v[168:171], v[120:123], v[200:203]\n\t"
      "ds_read_b64_tr_b16 v[156:157], v250 offset:1536\n\t"
      "ds_read_b64_tr_b16 v[158:159], v250 offset:5632\n\t"
      "v_mfma_f32_16x16x32_bf16 v[200:203], v[172:175], v[124:127], v[200:203]\n\t"
      "ds_read_b64_tr_b16 v[160:161], v249 offset:512\n\t"
      "v_mfma_f32_16x16x32_bf16 v[228:231], v[240:243], v[216:219], v[228:231]\n\t"
      "s_waitcnt lgkmcnt(9)\n\t"
      "ds_read_b64_tr_b16 v[162:163], v249 offset:4608\n\t"
      "ds_read_b64_tr_b16 v[164:165], v250 offset:512\n\t"
      "ds_read_b64_tr_b16 v[166:167], v250 offset:4608\n\t"
      "v_mfma_f32_16x16x32_bf16 v[0:3], v[176:179], v[216:219], v[0:3]\n\t"
      "v_exp_f32_e32 v194, v194\n\t"
      "v_mfma_f32_16x16x32_bf16 v[4:7], v[180:183], v[216:219], v[4:7]\n\t"
      "s_waitcnt lgkmcnt(8)\n\t"
      "v_add_u32_e32 v250, s34, v250\n\t"
      "v_exp_f32_e32 v195, v195\n\t"
      "v_mfma_f32_16x16x32_bf16 v[16:19], v[144:147], v[216:219], v[16:19]\n\t"
      "v_exp_f32_e32 v193, v193\n\t"
      "v_mfma_f32_16x16x32_bf16 v[20:23], v[148:151], v[216:219], v[20:23]\n\t"
      "s_waitcnt lgkmcnt(4)\n\t"
      "v_exp_f32_e32 v192, v192\n\t"
      "v_mfma_f32_16x16x32_bf16 v[24:27], v[152:155], v[216:219], v[24:27]\n\t"
      "v_exp_f32_e32 v197, v197\n\t"
      "v_mfma_f32_16x16x32_bf16 v[28:31], v[156:159], v[216:219], v[28:31]\n\t"
      "s_sleep 2\n\t"
      "s_waitcnt vmcnt(4) lgkmcnt(0)\n\t"
      "s_barrier\n\t"
      "v_mfma_f32_16x16x32_bf16 v[8:11], v[160:163], v[216:219], v[8:11]\n\t"
      "s_sub_u32 s30, s30, 1\n\t"
      "buffer_load_dwordx4 v251, s[12:15], s31 offen lds\n\t"
      "v_exp_f32_e32 v198, v198\n\t"
      "v_mfma_f32_16x16x32_bf16 v[12:15], v[164:167], v[216:219], v[12:15]\n\t"
      "s_addk_i32 m0, 896\n\t"
      "ds_read_b128 v[168:171], v248 offset:1024\n\t"
      "v_cvt_pk_bf16_f32 v217, v194, v195\n\t"
      "v_mfma_f32_16x16x32_bf16 v[40:43], v[160:163], v[220:223], v[40:43]\n\t"
      "buffer_load_dwordx4 v251, s[12:15], s31 offen offset:128 lds\n\t"
      "v_exp_f32_e32 v199, v199\n\t"
      "v_mfma_f32_16x16x32_bf16 v[44:47], v[164:167], v[220:223], v[44:47]\n\t"
      "s_addk_i32 m0, 7296\n\t"
      "ds_read_b128 v[172:175], v248 offset:1536\n\t"
      "v_add_u32_e32 v249, s34, v249\n\t"
      "v_mfma_f32_16x16x32_bf16 v[48:51], v[144:147], v[220:223], v[48:51]\n\t"
      "ds_read_b128 v[160:163], v248 offset:0\n\t"
      "ds_read_b128 v[164:167], v248 offset:512\n\t"
      "buffer_load_dwordx4 v252, s[16:19], s41 offen lds\n\t"
      "v_mfma_f32_16x16x32_bf16 v[52:55], v[148:151], v[220:223], v[52:55]\n\t"
      "s_addk_i32 m0, 896\n\t"
      "ds_read_b128 v[144:147], v248 offset:4096\n\t"
      "v_cvt_pk_bf16_f32 v216, v192, v193\n\t"
      "v_mfma_f32_16x16x32_bf16 v[56:59], v[152:155], v[220:223], v[56:59]\n\t"
      "buffer_load_dwordx4 v252, s[16:19], s41 offen offset:128 lds\n\t"
      "v_exp_f32_e32 v196, v196\n\t"
      "v_mfma_f32_16x16x32_bf16 v[60:63], v[156:159], v[220:223], v[60:63]\n\t"
      "s_mov_b32 s41, s31\n\t"
      "ds_read_b128 v[148:151], v248 offset:4608\n\t"
      "v_cvt_pk_bf16_f32 v218, v196, v197\n\t"
      "v_mfma_f32_16x16x32_bf16 v[32:35], v[176:179], v[220:223], v[32:35]\n\t"
      "s_add_u32 s31, s31, 0x20000\n\t"
      "ds_read_b128 v[152:155], v248 offset:5120\n\t"
      "ds_read_b128 v[156:159], v248 offset:5632\n\t"
      "v_mfma_f32_16x16x32_bf16 v[36:39], v[180:183], v[220:223], v[36:39]\n\t"
      "s_cmp_ge_u32 s30, s25\n\t"
      "v_exp_f32_e32 v207, v207\n\t"
      "v_mfma_f32_16x16x32_bf16 v[232:235], v[240:243], v[220:223], v[232:235]\n\t"
      "s_waitcnt lgkmcnt(4)\n\t"
      "s_cbranch_scc1 L_dm0_A2_%=\n\t"
      "s_cmp_lt_u32 s30, 3\n\t"
      "s_cbranch_scc1 L_s1_A2_%=\n\t"
      "L_dma_A2_%=:\n\t"
      "v_mfma_f32_16x16x32_bf16 v[192:195], v[160:163], v[96:99], v[184:187]\n\t"
      "s_cmp_lt_u32 s42, s40\n\t"
      "ds_read_b64_tr_b16 v[176:177], v249 offset:0\n\t"
      "v_exp_f32_e32 v204, v204\n\t"
      "v_mfma_f32_16x16x32_bf16 v[192:195], v[164:167], v[100:103], v[192:195]\n\t"
      "s_cselect_b32 s34, s35, s58\n\t"
      "ds_read_b64_tr_b16 v[178:179], v249 offset:4096\n\t"
      "v_add_u32_e32 v248, s34, v248\n\t"
      "v_mfma_f32_16x16x32_bf16 v[192:195], v[168:171], v[104:107], v[192:195]\n\t"
      "s_mov_b32 m0, s42\n\t"
      "ds_read_b64_tr_b16 v[180:181], v250 offset:0\n\t"
      "v_exp_f32_e32 v205, v205\n\t"
      "v_mfma_f32_16x16x32_bf16 v[192:195], v[172:175], v[108:111], v[192:195]\n\t"
      "s_waitcnt lgkmcnt(3)\n\t"
      "ds_read_b64_tr_b16 v[182:183], v250 offset:4096\n\t"
      "v_cvt_pk_bf16_f32 v219, v198, v199\n\t"
      "v_mfma_f32_16x16x32_bf16 v[196:199], v[144:147], v[96:99], v[184:187]\n\t"
      "s_add_u32 s42, s42, s34\n\t"
      "v_exp_f32_e32 v202, v202\n\t"
      "v_mfma_f32_16x16x32_bf16 v[196:199], v[148:151], v[100:103], v[196:199]\n\t"
      "v_exp_f32_e32 v201, v201\n\t"
      "v_mfma_f32_16x16x32_bf16 v[196:199], v[152:155], v[104:107], v[196:199]\n\t"
      "v_exp_f32_e32 v206, v206\n\t"
      "v_mfma_f32_16x16x32_bf16 v[196:199], v[156:159], v[108:111], v[196:199]\n\t"
      "v_cvt_pk_bf16_f32 v222, v204, v205\n\t"
      "v_cvt_pk_bf16_f32 v223, v206, v207\n\t"
      "v_mfma_f32_16x16x32_bf16 v[204:207], v[144:147], v[112:115], v[188:191]\n\t"
      "ds_read_b64_tr_b16 v[144:145], v249 offset:1024\n\t"
      "v_exp_f32_e32 v200, v200\n\t"
      "v_mfma_f32_16x16x32_bf16 v[204:207], v[148:151], v[116:119], v[204:207]\n\t"
      "ds_read_b64_tr_b16 v[146:147], v249 offset:5120\n\t"
      "v_cvt_pk_bf16_f32 v220, v200, v201\n\t"
      "v_mfma_f32_16x16x32_bf16 v[204:207], v[152:155], v[120:123], v[204:207]\n\t"
      "ds_read_b64_tr_b16 v[148:149], v250 offset:1024\n\t"
      "v_exp_f32_e32 v203, v203\n\t"
      "v_mfma_f32_16x16x32_bf16 v[204:207], v[156:159], v[124:127], v[204:207]\n\t"
      "ds_read_b64_tr_b16 v[150:151], v250 offset:5120\n\t"
      "v_cvt_pk_bf16_f32 v221, v202, v203\n\t"
      "v_mfma_f32_16x16x32_bf16 v[200:203], v[160:163], v[112:115], v[188:191]\n\t"
      "ds_read_b64_tr_b16 v[152:153], v249 offset:1536\n\t"
      "v_mfma_f32_16x16x32_bf16 v[200:203], v[164:167], v[116:119], v[200:203]\n\t"
      "ds_read_b64_tr_b16 v[154:155], v249 offset:5632\n\t"
      "v_mfma_f32_16x16x32_bf16 v[200:203], v[168:171], v[120:123], v[200:203]\n\t"
      "ds_read_b64_tr_b16 v[156:157], v250 offset:1536\n\t"
      "ds_read_b64_tr_b16 v[158:159], v250 offset:5632\n\t"
      "v_mfma_f32_16x16x32_bf16 v[200:203], v[172:175], v[124:127], v[200:203]\n\t"
      "ds_read_b64_tr_b16 v[160:161], v249 offset:512\n\t"
      "v_mfma_f32_16x16x32_bf16 v[228:231], v[240:243], v[216:219], v[228:231]\n\t"
      "s_waitcnt lgkmcnt(9)\n\t"
      "ds_read_b64_tr_b16 v[162:163], v249 offset:4608\n\t"
      "ds_read_b64_tr_b16 v[164:165], v250 offset:512\n\t"
      "ds_read_b64_tr_b16 v[166:167], v250 offset:4608\n\t"
      "v_mfma_f32_16x16x32_bf16 v[0:3], v[176:179], v[216:219], v[0:3]\n\t"
      "v_exp_f32_e32 v194, v194\n\t"
      "v_mfma_f32_16x16x32_bf16 v[4:7], v[180:183], v[216:219], v[4:7]\n\t"
      "s_waitcnt lgkmcnt(8)\n\t"
      "v_add_u32_e32 v250, s34, v250\n\t"
      "v_exp_f32_e32 v195, v195\n\t"
      "v_mfma_f32_16x16x32_bf16 v[16:19], v[144:147], v[216:219], v[16:19]\n\t"
      "v_exp_f32_e32 v193, v193\n\t"
      "v_mfma_f32_16x16x32_bf16 v[20:23], v[148:151], v[216:219], v[20:23]\n\t"
      "s_waitcnt lgkmcnt(4)\n\t"
      "v_exp_f32_e32 v192, v192\n\t"
      "v_mfma_f32_16x16x32_bf16 v[24:27], v[152:155], v[216:219], v[24:27]\n\t"
      "v_exp_f32_e32 v197, v197\n\t"
      "v_mfma_f32_16x16x32_bf16 v[28:31], v[156:159], v[216:219], v[28:31]\n\t"
      "s_waitcnt vmcnt(4) lgkmcnt(0)\n\t"
      "s_barrier\n\t"
      "v_mfma_f32_16x16x32_bf16 v[8:11], v[160:163], v[216:219], v[8:11]\n\t"
      "s_sub_u32 s30, s30, 1\n\t"
      "buffer_load_dwordx4 v251, s[12:15], s31 offen lds\n\t"
      "v_exp_f32_e32 v198, v198\n\t"
      "v_mfma_f32_16x16x32_bf16 v[12:15], v[164:167], v[216:219], v[12:15]\n\t"
      "s_addk_i32 m0, 896\n\t"
      "ds_read_b128 v[168:171], v248 offset:1024\n\t"
      "v_cvt_pk_bf16_f32 v217, v194, v195\n\t"
      "v_mfma_f32_16x16x32_bf16 v[40:43], v[160:163], v[220:223], v[40:43]\n\t"
      "buffer_load_dwordx4 v251, s[12:15], s31 offen offset:128 lds\n\t"
      "v_exp_f32_e32 v199, v199\n\t"
      "v_mfma_f32_16x16x32_bf16 v[44:47], v[164:167], v[220:223], v[44:47]\n\t"
      "s_addk_i32 m0, 7296\n\t"
      "ds_read_b128 v[172:175], v248 offset:1536\n\t"
      "v_add_u32_e32 v249, s34, v249\n\t"
      "v_mfma_f32_16x16x32_bf16 v[48:51], v[144:147], v[220:223], v[48:51]\n\t"
      "ds_read_b128 v[160:163], v248 offset:0\n\t"
      "ds_read_b128 v[164:167], v248 offset:512\n\t"
      "buffer_load_dwordx4 v252, s[16:19], s41 offen lds\n\t"
      "v_mfma_f32_16x16x32_bf16 v[52:55], v[148:151], v[220:223], v[52:55]\n\t"
      "s_addk_i32 m0, 896\n\t"
      "ds_read_b128 v[144:147], v248 offset:4096\n\t"
      "v_cvt_pk_bf16_f32 v216, v192, v193\n\t"
      "v_mfma_f32_16x16x32_bf16 v[56:59], v[152:155], v[220:223], v[56:59]\n\t"
      "buffer_load_dwordx4 v252, s[16:19], s41 offen offset:128 lds\n\t"
      "v_exp_f32_e32 v196, v196\n\t"
      "v_mfma_f32_16x16x32_bf16 v[60:63], v[156:159], v[220:223], v[60:63]\n\t"
      "s_mov_b32 s41, s31\n\t"
      "ds_read_b128 v[148:151], v248 offset:4608\n\t"
      "v_cvt_pk_bf16_f32 v218, v196, v197\n\t"
      "v_mfma_f32_16x16x32_bf16 v[32:35], v[176:179], v[220:223], v[32:35]\n\t"
      "s_add_u32 s31, s31, 0x20000\n\t"
      "ds_read_b128 v[152:155], v248 offset:5120\n\t"
      "ds_read_b128 v[156:159], v248 offset:5632\n\t"
      "v_mfma_f32_16x16x32_bf16 v[36:39], v[180:183], v[220:223], v[36:39]\n\t"
      "s_cmp_ge_u32 s30, s59\n\t"
      "v_exp_f32_e32 v207, v207\n\t"
      "v_mfma_f32_16x16x32_bf16 v[232:235], v[240:243], v[220:223], v[232:235]\n\t"
      "s_waitcnt lgkmcnt(4)\n\t"
      "s_cbranch_scc0 L_s1_A2_%=\n\t"
      "v_mfma_f32_16x16x32_bf16 v[192:195], v[160:163], v[96:99], v[184:187]\n\t"
      "s_cmp_lt_u32 s42, s40\n\t"
      "ds_read_b64_tr_b16 v[176:177], v249 offset:0\n\t"
      "v_exp_f32_e32 v204, v204\n\t"
      "v_mfma_f32_16x16x32_bf16 v[192:195], v[164:167], v[100:103], v[192:195]\n\t"
      "s_cselect_b32 s34, s35, s58\n\t"
      "ds_read_b64_tr_b16 v[178:179], v249 offset:4096\n\t"
      "v_add_u32_e32 v248, s34, v248\n\t"
      "v_mfma_f32_16x16x32_bf16 v[192:195], v[168:171], v[104:107], v[192:195]\n\t"
      "s_mov_b32 m0, s42\n\t"
      "ds_read_b64_tr_b16 v[180:181], v250 offset:0\n\t"
      "v_exp_f32_e32 v205, v205\n\t"
      "v_mfma_f32_16x16x32_bf16 v[192:195], v[172:175], v[108:111], v[192:195]\n\t"
      "s_waitcnt lgkmcnt(3)\n\t"
      "ds_read_b64_tr_b16 v[182:183], v250 offset:4096\n\t"
      "v_cvt_pk_bf16_f32 v219, v198, v199\n\t"
      "v_mfma_f32_16x16x32_bf16 v[196:199], v[144:147], v[96:99], v[184:187]\n\t"
      "s_add_u32 s42, s42, s34\n\t"
      "v_exp_f32_e32 v202, v202\n\t"
      "v_mfma_f32_16x16x32_bf16 v[196:199], v[148:151], v[100:103], v[196:199]\n\t"
      "v_exp_f32_e32 v201, v201\n\t"
      "v_mfma_f32_16x16x32_bf16 v[196:199], v[152:155], v[104:107], v[196:199]\n\t"
      "v_exp_f32_e32 v206, v206\n\t"
      "v_mfma_f32_16x16x32_bf16 v[196:199], v[156:159], v[108:111], v[196:199]\n\t"
      "v_cvt_pk_bf16_f32 v222, v204, v205\n\t"
      "v_cvt_pk_bf16_f32 v223, v206, v207\n\t"
      "v_mfma_f32_16x16x32_bf16 v[204:207], v[144:147], v[112:115], v[188:191]\n\t"
      "ds_read_b64_tr_b16 v[144:145], v249 offset:1024\n\t"
      "v_exp_f32_e32 v200, v200\n\t"
      "v_mfma_f32_16x16x32_bf16 v[204:207], v[148:151], v[116:119], v[204:207]\n\t"
      "ds_read_b64_tr_b16 v[146:147], v249 offset:5120\n\t"
      "v_cvt_pk_bf16_f32 v220, v200, v201\n\t"
      "v_mfma_f32_16x16x32_bf16 v[204:207], v[152:155], v[120:123], v[204:207]\n\t"
      "ds_read_b64_tr_b16 v[148:149], v250 offset:1024\n\t"
      "v_exp_f32_e32 v203, v203\n\t"
      "v_mfma_f32_16x16x32_bf16 v[204:207], v[156:159], v[124:127], v[204:207]\n\t"
      "ds_read_b64_tr_b16 v[150:151], v250 offset:5120\n\t"
      "v_cvt_pk_bf16_f32 v221, v202, v203\n\t"
      "v_mfma_f32_16x16x32_bf16 v[200:203], v[160:163], v[112:115], v[188:191]\n\t"
      "ds_read_b64_tr_b16 v[152:153], v249 offset:1536\n\t"
      "v_mfma_f32_16x16x32_bf16 v[200:203], v[164:167], v[116:119], v[200:203]\n\t"
      "ds_read_b64_tr_b16 v[154:155], v249 offset:5632\n\t"
      "v_mfma_f32_16x16x32_bf16 v[200:203], v[168:171], v[120:123], v[200:203]\n\t"
      "ds_read_b64_tr_b16 v[156:157], v250 offset:1536\n\t"
      "ds_read_b64_tr_b16 v[158:159], v250 offset:5632\n\t"
      "v_mfma_f32_16x16x32_bf16 v[200:203], v[172:175], v[124:127], v[200:203]\n\t"
      "ds_read_b64_tr_b16 v[160:161], v249 offset:512\n\t"
      "v_mfma_f32_16x16x32_bf16 v[228:231], v[240:243], v[216:219], v[228:231]\n\t"
      "s_waitcnt lgkmcnt(9)\n\t"
      "ds_read_b64_tr_b16 v[162:163], v249 offset:4608\n\t"
      "ds_read_b64_tr_b16 v[164:165], v250 offset:512\n\t"
      "ds_read_b64_tr_b16 v[166:167], v250 offset:4608\n\t"
      "v_mfma_f32_16x16x32_bf16 v[0:3], v[176:179], v[216:219], v[0:3]\n\t"
      "v_exp_f32_e32 v194, v194\n\t"
      "v_mfma_f32_16x16x32_bf16 v[4:7], v[180:183], v[216:219], v[4:7]\n\t"
      "s_waitcnt lgkmcnt(8)\n\t"
      "v_add_u32_e32 v250, s34, v250\n\t"
      "v_exp_f32_e32 v195, v195\n\t"
      "v_mfma_f32_16x16x32_bf16 v[16:19], v[144:147], v[216:219], v[16:19]\n\t"
      "v_exp_f32_e32 v193, v193\n\t"
      "v_mfma_f32_16x16x32_bf16 v[20:23], v[148:151], v[216:219], v[20:23]\n\t"
      "s_waitcnt lgkmcnt(4)\n\t"
      "v_exp_f32_e32 v192, v192\n\t"
      "v_mfma_f32_16x16x32_bf16 v[24:27], v[152:155], v[216:219], v[24:27]\n\t"
      "v_exp_f32_e32 v197, v197\n\t"
      "v_mfma_f32_16x16x32_bf16 v[28:31], v[156:159], v[216:219], v[28:31]\n\t"
      "s_waitcnt vmcnt(4) lgkmcnt(0)\n\t"
      "s_barrier\n\t"
      "v_mfma_f32_16x16x32_bf16 v[8:11], v[160:163], v[216:219], v[8:11]\n\t"
      "s_sub_u32 s30, s30, 1\n\t"
      "buffer_load_dwordx4 v251, s[12:15], s31 offen lds\n\t"
      "v_exp_f32_e32 v198, v198\n\t"
      "v_mfma_f32_16x16x32_bf16 v[12:15], v[164:167], v[216:219], v[12:15]\n\t"
      "s_addk_i32 m0, 896\n\t"
      "ds_read_b128 v[168:171], v248 offset:1024\n\t"
      "v_cvt_pk_bf16_f32 v217, v194, v195\n\t"
      "v_mfma_f32_16x16x32_bf16 v[40:43], v[160:163], v[220:223], v[40:43]\n\t"
      "buffer_load_dwordx4 v251, s[12:15], s31 offen offset:128 lds\n\t"
      "v_exp_f32_e32 v199, v199\n\t"
      "v_mfma_f32_16x16x32_bf16 v[44:47], v[164:167], v[220:223], v[44:47]\n\t"
      "s_addk_i32 m0, 7296\n\t"
      "ds_read_b128 v[172:175], v248 offset:1536\n\t"
      "v_add_u32_e32 v249, s34, v249\n\t"
      "v_mfma_f32_16x16x32_bf16 v[48:51], v[144:147], v[220:223], v[48:51]\n\t"
      "ds_read_b128 v[160:163], v248 offset:0\n\t"
      "ds_read_b128 v[164:167], v248 offset:512\n\t"
      "buffer_load_dwordx4 v252, s[16:19], s41 offen lds\n\t"
      "v_mfma_f32_16x16x32_bf16 v[52:55], v[148:151], v[220:223], v[52:55]\n\t"
      "s_addk_i32 m0, 896\n\t"
      "ds_read_b128 v[144:147], v248 offset:4096\n\t"
      "v_cvt_pk_bf16_f32 v216, v192, v193\n\t"
      "v_mfma_f32_16x16x32_bf16 v[56:59], v[152:155], v[220:223], v[56:59]\n\t"
      "buffer_load_dwordx4 v252, s[16:19], s41 offen offset:128 lds\n\t"
      "v_exp_f32_e32 v196, v196\n\t"
      "v_mfma_f32_16x16x32_bf16 v[60:63], v[156:159], v[220:223], v[60:63]\n\t"
      "s_mov_b32 s41, s31\n\t"
      "ds_read_b128 v[148:151], v248 offset:4608\n\t"
      "v_cvt_pk_bf16_f32 v218, v196, v197\n\t"
      "v_mfma_f32_16x16x32_bf16 v[32:35], v[176:179], v[220:223], v[32:35]\n\t"
      "s_add_u32 s31, s31, 0x20000\n\t"
      "ds_read_b128 v[152:155], v248 offset:5120\n\t"
      "ds_read_b128 v[156:159], v248 offset:5632\n\t"
      "v_mfma_f32_16x16x32_bf16 v[36:39], v[180:183], v[220:223], v[36:39]\n\t"
      "s_cmp_ge_u32 s30, s59\n\t"
      "v_exp_f32_e32 v207, v207\n\t"
      "v_mfma_f32_16x16x32_bf16 v[232:235], v[240:243], v[220:223], v[232:235]\n\t"
      "s_waitcnt lgkmcnt(4)\n\t"
      "s_cbranch_scc1 L_dma_A2_%=\n\t"
      "L_s1_A2_%=:\n\t"
      "v_mbcnt_lo_u32_b32 v252, -1, 0\n\t"
      "v_mbcnt_hi_u32_b32 v252, -1, v252\n\t"
      "v_and_b32_e32 v253, 7, v252\n\t"
      "v_bfe_u32 v251, v252, 3, 1\n\t"
      "v_lshl_or_b32 v253, v253, 1, v251\n\t"
      "v_lshrrev_b32_e32 v251, 4, v252\n\t"
      "v_xor_b32_e32 v253, v253, v251\n\t"
      "v_and_b32_e32 v251, 15, v252\n\t"
      "v_lshlrev_b32_e32 v251, 4, v251\n\t"
      "v_add_lshl_u32 v253, v253, v251, 3\n\t"
      "v_lshrrev_b32_e32 v251, 3, v252\n\t"
      "s_movk_i32 s37, 0xf8\n\t"
      "v_mad_u32_u24 v254, v251, s37, v252\n\t"
      "v_xor_b32_e32 v251, v251, v252\n\t"
      "v_lshlrev_b32_e32 v251, 4, v251\n\t"
      "v_lshlrev_b32_e32 v252, 4, v254\n\t"
      "s_cmp_eq_u32 s30, 0\n\t"
      "s_cbranch_scc1 L_dr_A2_%=\n\t"
      "L_nod_A2_%=:\n\t"
      "v_mfma_f32_16x16x32_bf16 v[192:195], v[160:163], v[96:99], v[184:187]\n\t"
      "s_cmp_lt_u32 s42, s40\n\t"
      "ds_read_b64_tr_b16 v[176:177], v249 offset:0\n\t"
      "v_exp_f32_e32 v204, v204\n\t"
      "v_mfma_f32_16x16x32_bf16 v[192:195], v[164:167], v[100:103], v[192:195]\n\t"
      "s_cselect_b32 s34, s35, s58\n\t"
      "ds_read_b64_tr_b16 v[178:179], v249 offset:4096\n\t"
      "v_add_u32_e32 v248, s34, v248\n\t"
      "v_mfma_f32_16x16x32_bf16 v[192:195], v[168:171], v[104:107], v[192:195]\n\t"
      "s_mov_b32 m0, s42\n\t"
      "ds_read_b64_tr_b16 v[180:181], v250 offset:0\n\t"
      "v_exp_f32_e32 v205, v205\n\t"
      "v_mfma_f32_16x16x32_bf16 v[192:195], v[172:175], v[108:111], v[192:195]\n\t"
      "s_waitcnt lgkmcnt(3)\n\t"
      "ds_read_b64_tr_b16 v[182:183], v250 offset:4096\n\t"
      "v_cvt_pk_bf16_f32 v219, v198, v199\n\t"
      "v_mfma_f32_16x16x32_bf16 v[196:199], v[144:147], v[96:99], v[184:187]\n\t"
      "s_add_u32 s42, s42, s34\n\t"
      "v_exp_f32_e32 v202, v202\n\t"
      "v_mfma_f32_16x16x32_bf16 v[196:199], v[148:151], v[100:103], v[196:199]\n\t"
      "v_exp_f32_e32 v201, v201\n\t"
      "v_mfma_f32_16x16x32_bf16 v[196:199], v[152:155], v[104:107], v[196:199]\n\t"
      "v_exp_f32_e32 v206, v206\n\t"
      "v_mfma_f32_16x16x32_bf16 v[196:199], v[156:159], v[108:111], v[196:199]\n\t"
      "v_cvt_pk_bf16_f32 v222, v204, v205\n\t"
      "v_cvt_pk_bf16_f32 v223, v206, v207\n\t"
      "v_mfma_f32_16x16x32_bf16 v[204:207], v[144:147], v[112:115], v[188:191]\n\t"
      "ds_read_b64_tr_b16 v[144:145], v249 offset:1024\n\t"
      "v_exp_f32_e32 v200, v200\n\t"
      "v_mfma_f32_16x16x32_bf16 v[204:207], v[148:151], v[116:119], v[204:207]\n\t"
      "ds_read_b64_tr_b16 v[146:147], v249 offset:5120\n\t"
      "v_cvt_pk_bf16_f32 v220, v200, v201\n\t"
      "v_mfma_f32_16x16x32_bf16 v[204:207], v[152:155], v[120:123], v[204:207]\n\t"
      "ds_read_b64_tr_b16 v[148:149], v250 offset:1024\n\t"
      "v_exp_f32_e32 v203, v203\n\t"
      "v_mfma_f32_16x16x32_bf16 v[204:207], v[156:159], v[124:127], v[204:207]\n\t"
      "ds_read_b64_tr_b16 v[150:151], v250 offset:5120\n\t"
      "v_cvt_pk_bf16_f32 v221, v202, v203\n\t"
      "v_mfma_f32_16x16x32_bf16 v[200:203], v[160:163], v[112:115], v[188:191]\n\t"
      "ds_read_b64_tr_b16 v[152:153], v249 offset:1536\n\t"
      "v_mfma_f32_16x16x32_bf16 v[200:203], v[164:167], v[116:119], v[200:203]\n\t"
      "ds_read_b64_tr_b16 v[154:155], v249 offset:5632\n\t"
      "v_mfma_f32_16x16x32_bf16 v[200:203], v[168:171], v[120:123], v[200:203]\n\t"
      "ds_read_b64_tr_b16 v[156:157], v250 offset:1536\n\t"
      "ds_read_b64_tr_b16 v[158:159], v250 offset:5632\n\t"
      "v_mfma_f32_16x16x32_bf16 v[200:203], v[172:175], v[124:127], v[200:203]\n\t"
      "ds_read_b64_tr_b16 v[160:161], v249 offset:512\n\t"
      "v_mfma_f32_16x16x32_bf16 v[228:231], v[240:243], v[216:219], v[228:231]\n\t"
      "s_waitcnt lgkmcnt(9)\n\t"
      "ds_read_b64_tr_b16 v[162:163], v249 offset:4608\n\t"
      "ds_read_b64_tr_b16 v[164:165], v250 offset:512\n\t"
      "ds_read_b64_tr_b16 v[166:167], v250 offset:4608\n\t"
      "v_mfma_f32_16x16x32_bf16 v[0:3], v[176:179], v[216:219], v[0:3]\n\t"
      "v_exp_f32_e32 v194, v194\n\t"
      "v_mfma_f32_16x16x32_bf16 v[4:7], v[180:183], v[216:219], v[4:7]\n\t"
      "s_waitcnt lgkmcnt(8)\n\t"
      "v_add_u32_e32 v250, s34, v250\n\t"
      "v_exp_f32_e32 v195, v195\n\t"
      "v_mfma_f32_16x16x32_bf16 v[16:19], v[144:147], v[216:219], v[16:19]\n\t"
      "v_exp_f32_e32 v193, v193\n\t"
      "v_mfma_f32_16x16x32_bf16 v[20:23], v[148:151], v[216:219], v[20:23]\n\t"
      "s_waitcnt lgkmcnt(4)\n\t"
      "v_exp_f32_e32 v192, v192\n\t"
      "v_mfma_f32_16x16x32_bf16 v[24:27], v[152:155], v[216:219], v[24:27]\n\t"
      "v_exp_f32_e32 v197, v197\n\t"
      "v_mfma_f32_16x16x32_bf16 v[28:31], v[156:159], v[216:219], v[28:31]\n\t"
      "s_cmp_eq_u32 s30, 2\n\t"
      "s_cbranch_scc1 L_nod_A2_%=_w\n\t"
      "s_waitcnt vmcnt(0)\n\t"
      "L_nod_A2_%=_w:\n\t"
      "s_waitcnt vmcnt(4) lgkmcnt(0)\n\t"
      "s_barrier\n\t"
      "v_mfma_f32_16x16x32_bf16 v[8:11], v[160:163], v[216:219], v[8:11]\n\t"
      "s_sub_u32 s30, s30, 1\n\t"
      "v_exp_f32_e32 v198, v198\n\t"
      "v_mfma_f32_16x16x32_bf16 v[12:15], v[164:167], v[216:219], v[12:15]\n\t"
      "ds_read_b128 v[168:171], v248 offset:1024\n\t"
      "v_cvt_pk_bf16_f32 v217, v194, v195\n\t"
      "v_mfma_f32_16x16x32_bf16 v[40:43], v[160:163], v[220:223], v[40:43]\n\t"
      "v_exp_f32_e32 v199, v199\n\t"
      "v_mfma_f32_16x16x32_bf16 v[44:47], v[164:167], v[220:223], v[44:47]\n\t"
      "ds_read_b128 v[172:175], v248 offset:1536\n\t"
      "v_add_u32_e32 v249, s34, v249\n\t"
      "v_mfma_f32_16x16x32_bf16 v[48:51], v[144:147], v[220:223], v[48:51]\n\t"
      "ds_read_b128 v[160:163], v248 offset:0\n\t"
      "ds_read_b128 v[164:167], v248 offset:512\n\t"
      "v_mfma_f32_16x16x32_bf16 v[52:55], v[148:151], v[220:223], v[52:55]\n\t"
      "ds_read_b128 v[144:147], v248 offset:4096\n\t"
      "v_cvt_pk_bf16_f32 v216, v192, v193\n\t"
      "v_mfma_f32_16x16x32_bf16 v[56:59], v[152:155], v[220:223], v[56:59]\n\t"
      "v_exp_f32_e32 v196, v196\n\t"
      "v_mfma_f32_16x16x32_bf16 v[60:63], v[156:159], v[220:223], v[60:63]\n\t"
      "ds_read_b128 v[148:151], v248 offset:4608\n\t"
      "v_cvt_pk_bf16_f32 v218, v196, v197\n\t"
      "v_mfma_f32_16x16x32_bf16 v[32:35], v[176:179], v[220:223], v[32:35]\n\t"
      "ds_read_b128 v[152:155], v248 offset:5120\n\t"
      "ds_read_b128 v[156:159], v248 offset:5632\n\t"
      "v_mfma_f32_16x16x32_bf16 v[36:39], v[180:183], v[220:223], v[36:39]\n\t"
      "s_cmp_ge_u32 s30, 1\n\t"
      "v_exp_f32_e32 v207, v207\n\t"
      "v_mfma_f32_16x16x32_bf16 v[232:235], v[240:243], v[220:223], v[232:235]\n\t"
      "s_waitcnt lgkmcnt(4)\n\t"
      "s_cbranch_scc1 L_nod_A2_%=\n\t"
      "L_dr_A2_%=:\n\t"
      "ds_read_b64_tr_b16 v[176:177], v249 offset:0\n\t"
      "ds_read_b64_tr_b16 v[178:179], v249 offset:4096\n\t"
      "ds_read_b64_tr_b16 v[180:181], v250 offset:0\n\t"
      "ds_read_b64_tr_b16 v[182:183], v250 offset:4096\n\t"
      "ds_read_b64_tr_b16 v[160:161], v249 offset:512\n\t"
      "ds_read_b64_tr_b16 v[162:163], v249 offset:4608\n\t"
      "ds_read_b64_tr_b16 v[164:165], v250 offset:512\n\t"
      "ds_read_b64_tr_b16 v[166:167], v250 offset:4608\n\t"
      "ds_read_b64_tr_b16 v[144:145], v249 offset:1024\n\t"
      "ds_read_b64_tr_b16 v[146:147], v249 offset:5120\n\t"
      "ds_read_b64_tr_b16 v[148:149], v250 offset:1024\n\t"
      "v_cvt_pk_bf16_f32 v219, v198, v199\n\t"
      "v_exp_f32_e32 v204, v204\n\t"
      "s_nop 0\n\t"
      "v_mfma_f32_16x16x32_bf16 v[228:231], v[240:243], v[216:219], v[228:231]\n\t"
      "ds_read_b64_tr_b16 v[150:151], v250 offset:5120\n\t"
      "v_exp_f32_e32 v205, v205\n\t"
      "v_exp_f32_e32 v202, v202\n\t"
      "v_exp_f32_e32 v201, v201\n\t"
      "v_exp_f32_e32 v206, v206\n\t"
      "v_exp_f32_e32 v200, v200\n\t"
      "v_exp_f32_e32 v203, v203\n\t"
      "s_waitcnt lgkmcnt(8)\n\t"
      "v_mfma_f32_16x16x32_bf16 v[0:3], v[176:179], v[216:219], v[0:3]\n\t"
      "ds_read_b64_tr_b16 v[152:153], v249 offset:1536\n\t"
      "v_cvt_pk_bf16_f32 v222, v204, v205\n\t"
      "v_mfma_f32_16x16x32_bf16 v[4:7], v[180:183], v[216:219], v[4:7]\n\t"
      "ds_read_b64_tr_b16 v[154:155], v249 offset:5632\n\t"
      "v_cvt_pk_bf16_f32 v223, v206, v207\n\t"
      "s_waitcnt lgkmcnt(8)\n\t"
      "v_mfma_f32_16x16x32_bf16 v[8:11], v[160:163], v[216:219], v[8:11]\n\t"
      "ds_read_b64_tr_b16 v[156:157], v250 offset:1536\n\t"
      "v_cvt_pk_bf16_f32 v220, v200, v201\n\t"
      "s_waitcnt lgkmcnt(4)\n\t"
      "v_mfma_f32_16x16x32_bf16 v[12:15], v[164:167], v[216:219], v[12:15]\n\t"
      "ds_read_b64_tr_b16 v[158:159], v250 offset:5632\n\t"
      "v_cvt_pk_bf16_f32 v221, v202, v203\n\t"
      "v_mfma_f32_16x16x32_bf16 v[16:19], v[144:147], v[216:219], v[16:19]\n\t"
      "s_lshr_b32 s37, s42, 14\n\t"
      "v_rcp_f32_e32 v230, v228\n\t"
      "v_mfma_f32_16x16x32_bf16 v[232:235], v[240:243], v[220:223], v[232:235]\n\t"
      "s_add_u32 s37, s37, s26\n\t"
      "v_mul_f32_e32 v0, v230, v0\n\t"
      "v_mul_f32_e32 v1, v230, v1\n\t"
      "s_waitcnt lgkmcnt(4)\n\t"
      "v_mfma_f32_16x16x32_bf16 v[20:23], v[148:151], v[216:219], v[20:23]\n\t"
      "s_add_u32 s37, s37, 1\n\t"
      "v_mul_f32_e32 v2, v230, v2\n\t"
      "v_mul_f32_e32 v3, v230, v3\n\t"
      "v_mfma_f32_16x16x32_bf16 v[32:35], v[176:179], v[220:223], v[32:35]\n\t"
      "s_sub_u32 s38, s37, 5\n\t"
      "s_cselect_b32 s37, s37, s38\n\t"
      "v_mul_f32_e32 v4, v230, v4\n\t"
      "v_mul_f32_e32 v5, v230, v5\n\t"
      "v_mfma_f32_16x16x32_bf16 v[36:39], v[180:183], v[220:223], v[36:39]\n\t"
      "s_lshl_b32 s42, s37, 14\n\t"
      "v_cvt_pk_bf16_f32 v0, v0, v1\n\t"
      "v_cvt_pk_bf16_f32 v1, v2, v3\n\t"
      "s_waitcnt lgkmcnt(0)\n\t"
      "v_mfma_f32_16x16x32_bf16 v[24:27], v[152:155], v[216:219], v[24:27]\n\t"
      "v_mul_f32_e32 v6, v230, v6\n\t"
      "v_mul_f32_e32 v7, v230, v7\n\t"
      "v_mfma_f32_16x16x32_bf16 v[40:43], v[160:163], v[220:223], v[40:43]\n\t"
      "v_mul_f32_e32 v8, v230, v8\n\t"
      "v_mul_f32_e32 v9, v230, v9\n\t"
      "v_mfma_f32_16x16x32_bf16 v[28:31], v[156:159], v[216:219], v[28:31]\n\t"
      "v_cvt_pk_bf16_f32 v4, v4, v5\n\t"
      "v_cvt_pk_bf16_f32 v5, v6, v7\n\t"
      "v_mfma_f32_16x16x32_bf16 v[44:47], v[164:167], v[220:223], v[44:47]\n\t"
      "v_mul_f32_e32 v10, v230, v10\n\t"
      "v_mul_f32_e32 v11, v230, v11\n\t"
      "v_mfma_f32_16x16x32_bf16 v[48:51], v[144:147], v[220:223], v[48:51]\n\t"
      "v_mul_f32_e32 v12, v230, v12\n\t"
      "v_mul_f32_e32 v13, v230, v13\n\t"
      "v_mfma_f32_16x16x32_bf16 v[52:55], v[148:151], v[220:223], v[52:55]\n\t"
      "v_cvt_pk_bf16_f32 v8, v8, v9\n\t"
      "v_cvt_pk_bf16_f32 v9, v10, v11\n\t"
      "v_mfma_f32_16x16x32_bf16 v[56:59], v[152:155], v[220:223], v[56:59]\n\t"
      "v_mul_f32_e32 v14, v230, v14\n\t"
      "v_mul_f32_e32 v15, v230, v15\n\t"
      "v_mfma_f32_16x16x32_bf16 v[60:63], v[156:159], v[220:223], v[60:63]\n\t"
      "v_cvt_pk_bf16_f32 v12, v12, v13\n\t"
      "v_cvt_pk_bf16_f32 v13, v14, v15\n\t"
      "v_add_u32_e32 v253, s42, v253\n\t"
      "v_add_u32_e32 v251, s42, v251\n\t"
      "v_xor_b32_e32 v248, 32, v253\n\t"
      "v_xor_b32_e32 v254, 64, v253\n\t"
      "v_xor_b32_e32 v143, 96, v253\n\t"
      "ds_write_b64 v253, v[0:1] offset:0\n\t"
      "ds_write_b64 v248, v[4:5] offset:0\n\t"
      "ds_write_b64 v254, v[8:9] offset:0\n\t"
      "ds_write_b64 v143, v[12:13] offset:0\n\t"
      "ds_read_b128 v[96:99], v251 offset:0\n\t"
      "ds_read_b128 v[100:103], v251 offset:1024\n\t"
      "v_rcp_f32_e32 v234, v232\n\t"
      "v_mul_f32_e32 v16, v230, v16\n\t"
      "v_mul_f32_e32 v17, v230, v17\n\t"
      "v_add_f32_e32 v229, v228, v232\n\t"
      "v_add_f32_e32 v229, v229, v230\n\t"
      "v_add_f32_e32 v229, v229, v234\n\t"
      "v_cmp_nge_f32_e32 vcc, 0x4f800000, v229\n\t"
      "v_mul_f32_e32 v18, v230, v18\n\t"
      "v_mul_f32_e32 v19, v230, v19\n\t"
      "s_mov_b32 s39, 1\n\t"
      "s_cbranch_vccnz L_end_%=\n\t"
      "s_mov_b32 s39, 0\n\t"
      "v_mul_f32_e32 v20, v230, v20\n\t"
      "v_mul_f32_e32 v21, v230, v21\n\t"
      "v_cvt_pk_bf16_f32 v16, v16, v17\n\t"
      "v_cvt_pk_bf16_f32 v17, v18, v19\n\t"
      "v_mul_f32_e32 v22, v230, v22\n\t"
      "v_mul_f32_e32 v23, v230, v23\n\t"
      "v_mul_f32_e32 v24, v230, v24\n\t"
      "v_mul_f32_e32 v25, v230, v25\n\t"
      "v_cvt_pk_bf16_f32 v20, v20, v21\n\t"
      "v_cvt_pk_bf16_f32 v21, v22, v23\n\t"
      "s_waitcnt lgkmcnt(1)\n\t"
      "buffer_store_dwordx4 v[96:99], v252, s[20:23], s27 offen nt sc1\n\t"
      "s_add_u32 s38, s27, 0x8000\n\t"
      "s_waitcnt lgkmcnt(0)\n\t"
      "v_mov_b32_e32 v104, v100\n\t"
      "v_mov_b32_e32 v105, v101\n\t"
      "buffer_store_dwordx4 v[102:105], v252, s[20:23], s38 offen nt sc1\n\t"
      "v_mul_f32_e32 v26, v230, v26\n\t"
      "v_mul_f32_e32 v27, v230, v27\n\t"
      "v_mul_f32_e32 v28, v230, v28\n\t"
      "v_mul_f32_e32 v29, v230, v29\n\t"
      "v_cvt_pk_bf16_f32 v24, v24, v25\n\t"
      "v_cvt_pk_bf16_f32 v25, v26, v27\n\t"
      "v_mul_f32_e32 v30, v230, v30\n\t"
      "v_mul_f32_e32 v31, v230, v31\n\t"
      "v_cvt_pk_bf16_f32 v28, v28, v29\n\t"
      "v_cvt_pk_bf16_f32 v29, v30, v31\n\t"
      "ds_write_b64 v253, v[16:17] offset:2048\n\t"
      "ds_write_b64 v248, v[20:21] offset:2048\n\t"
      "ds_write_b64 v254, v[24:25] offset:2048\n\t"
      "ds_write_b64 v143, v[28:29] offset:2048\n\t"
      "ds_read_b128 v[106:109], v251 offset:2048\n\t"
      "ds_read_b128 v[110:113], v251 offset:3072\n\t"
      "v_mul_f32_e32 v32, v234, v32\n\t"
      "v_mul_f32_e32 v33, v234, v33\n\t"
      "v_mul_f32_e32 v34, v234, v34\n\t"
      "v_mul_f32_e32 v35, v234, v35\n\t"
      "v_mul_f32_e32 v36, v234, v36\n\t"
      "v_mul_f32_e32 v37, v234, v37\n\t"
      "v_cvt_pk_bf16_f32 v32, v32, v33\n\t"
      "v_cvt_pk_bf16_f32 v33, v34, v35\n\t"
      "v_mul_f32_e32 v38, v234, v38\n\t"
      "v_mul_f32_e32 v39, v234, v39\n\t"
      "v_mul_f32_e32 v40, v234, v40\n\t"
      "v_mul_f32_e32 v41, v234, v41\n\t"
      "v_cvt_pk_bf16_f32 v36, v36, v37\n\t"
      "v_cvt_pk_bf16_f32 v37, v38, v39\n\t"
      "v_mul_f32_e32 v42, v234, v42\n\t"
      "v_mul_f32_e32 v43, v234, v43\n\t"
      "v_mul_f32_e32 v44, v234, v44\n\t"
      "v_mul_f32_e32 v45, v234, v45\n\t"
      "v_cvt_pk_bf16_f32 v40, v40, v41\n\t"
      "v_cvt_pk_bf16_f32 v41, v42, v43\n\t"
      "v_mul_f32_e32 v46, v234, v46\n\t"
      "v_mul_f32_e32 v47, v234, v47\n\t"
      "s_waitcnt lgkmcnt(1)\n\t"
      "buffer_store_dwordx4 v[106:109], v252, s[20:23], s27 offen offset:128 nt sc1\n\t"
      "s_waitcnt lgkmcnt(0)\n\t"
      "v_mov_b32_e32 v114, v110\n\t"
      "v_mov_b32_e32 v115, v111\n\t"
      "buffer_store_dwordx4 v[112:115], v252, s[20:23], s38 offen offset:128 nt sc1\n\t"
      "s_add_u32 s27, s27, 0x10000\n\t"
      "v_cvt_pk_bf16_f32 v44, v44, v45\n\t"
      "v_cvt_pk_bf16_f32 v45, v46, v47\n\t"
      "ds_write_b64 v253, v[32:33] offset:4096\n\t"
      "ds_write_b64 v248, v[36:37] offset:4096\n\t"
      "ds_write_b64 v254, v[40:41] offset:4096\n\t"
      "ds_write_b64 v143, v[44:45] offset:4096\n\t"
      "ds_read_b128 v[116:119], v251 offset:4096\n\t"
      "ds_read_b128 v[120:123], v251 offset:5120\n\t"
      "v_mul_f32_e32 v48, v234, v48\n\t"
      "v_mul_f32_e32 v49, v234, v49\n\t"
      "v_mul_f32_e32 v50, v234, v50\n\t"
      "v_mul_f32_e32 v51, v234, v51\n\t"
      "v_mul_f32_e32 v52, v234, v52\n\t"
      "v_mul_f32_e32 v53, v234, v53\n\t"
      "v_cvt_pk_bf16_f32 v48, v48, v49\n\t"
      "v_cvt_pk_bf16_f32 v49, v50, v51\n\t"
      "v_mul_f32_e32 v54, v234, v54\n\t"
      "v_mul_f32_e32 v55, v234, v55\n\t"
      "v_mul_f32_e32 v56, v234, v56\n\t"
      "v_mul_f32_e32 v57, v234, v57\n\t"
      "v_cvt_pk_bf16_f32 v52, v52, v53\n\t"
      "v_cvt_pk_bf16_f32 v53, v54, v55\n\t"
      "v_mul_f32_e32 v58, v234, v58\n\t"
      "v_mul_f32_e32 v59, v234, v59\n\t"
      "v_mul_f32_e32 v60, v234, v60\n\t"
      "v_mul_f32_e32 v61, v234, v61\n\t"
      "v_cvt_pk_bf16_f32 v56, v56, v57\n\t"
      "v_cvt_pk_bf16_f32 v57, v58, v59\n\t"
      "v_mul_f32_e32 v62, v234, v62\n\t"
      "v_mul_f32_e32 v63, v234, v63\n\t"
      "s_waitcnt lgkmcnt(1)\n\t"
      "buffer_store_dwordx4 v[116:119], v252, s[20:23], s27 offen nt sc1\n\t"
      "s_add_u32 s38, s27, 0x8000\n\t"
      "s_waitcnt lgkmcnt(0)\n\t"
      "v_mov_b32_e32 v124, v120\n\t"
      "v_mov_b32_e32 v125, v121\n\t"
      "buffer_store_dwordx4 v[122:125], v252, s[20:23], s38 offen nt sc1\n\t"
      "v_cvt_pk_bf16_f32 v60, v60, v61\n\t"
      "v_cvt_pk_bf16_f32 v61, v62, v63\n\t"
      "ds_write_b64 v253, v[48:49] offset:6144\n\t"
      "ds_write_b64 v248, v[52:53] offset:6144\n\t"
      "ds_write_b64 v254, v[56:57] offset:6144\n\t"
      "ds_write_b64 v143, v[60:61] offset:6144\n\t"
      "ds_read_b128 v[126:129], v251 offset:6144\n\t"
      "ds_read_b128 v[130:133], v251 offset:7168\n\t"
      "s_waitcnt lgkmcnt(1)\n\t"
      "buffer_store_dwordx4 v[126:129], v252, s[20:23], s27 offen offset:128 nt sc1\n\t"
      "s_waitcnt lgkmcnt(0)\n\t"
      "v_mov_b32_e32 v134, v130\n\t"
      "v_mov_b32_e32 v135, v131\n\t"
      "buffer_store_dwordx4 v[132:135], v252, s[20:23], s38 offen offset:128 nt sc1\n\t"
      "s_add_u32 s27, s27, 0x10000\n\t"
      "s_add_u32 s37, s7, 16\n\t"
      "s_cmp_lt_u32 s37, s54\n\t"
      "s_cbranch_scc1 L_end_%=\n\t"
      "s_cmp_lg_u32 s43, 0\n\t"
      "s_cbranch_scc1 L_end_%=\n\t"
      "s_endpgm\n\t"
      "L_B_%=:\n\t"
      "s_cbranch_scc0 L_B3_%=\n\t"
      "L_B2_%=:\n\t"
      "s_waitcnt vmcnt(12)\n\t"
      "s_barrier\n\t"
      "ds_read_b128 v[160:163], v248 offset:0\n\t"
      "ds_read_b128 v[164:167], v248 offset:512\n\t"
      "ds_read_b128 v[168:171], v248 offset:1024\n\t"
      "ds_read_b128 v[172:175], v248 offset:1536\n\t"
      "ds_read_b128 v[144:147], v248 offset:4096\n\t"
      "ds_read_b128 v[148:151], v248 offset:4608\n\t"
      "ds_read_b128 v[152:155], v248 offset:5120\n\t"
      "ds_read_b128 v[156:159], v248 offset:5632\n\t"
      "s_waitcnt vmcnt(8)\n\t"
      "ds_read_b128 v[96:99], v254 offset:0\n\t"
      "ds_read_b128 v[100:103], v254 offset:512\n\t"
      "ds_read_b128 v[104:107], v254 offset:1024\n\t"
      "ds_read_b128 v[108:111], v254 offset:1536\n\t"
      "s_waitcnt lgkmcnt(0)\n\t"
      "s_add_u32 m0, s42, 32768\n\t"
      "s_nop 0\n\t"
      "buffer_load_dwordx4 v251, s[12:15], s31 offen lds\n\t"
      "s_addk_i32 m0, 896\n\t"
      "s_nop 0\n\t"
      "buffer_load_dwordx4 v251, s[12:15], s31 offen offset:128 lds\n\t"
      "s_addk_i32 m0, 7296\n\t"
      "s_nop 0\n\t"
      "buffer_load_dwordx4 v252, s[16:19], s41 offen lds\n\t"
      "s_addk_i32 m0, 896\n\t"
      "s_nop 0\n\t"
      "buffer_load_dwordx4 v252, s[16:19], s41 offen offset:128 lds\n\t"
      "s_mov_b32 s41, s31\n\t"
      "s_add_u32 s31, s31, 0x20000\n\t"
      "v_mfma_f32_16x16x32_bf16 v[192:195], v[160:163], v[96:99], v[176:179]\n\t"
      "v_mfma_f32_16x16x32_bf16 v[192:195], v[164:167], v[100:103], v[192:195]\n\t"
      "v_mfma_f32_16x16x32_bf16 v[192:195], v[168:171], v[104:107], v[192:195]\n\t"
      "v_mfma_f32_16x16x32_bf16 v[192:195], v[172:175], v[108:111], v[192:195]\n\t"
      "v_mfma_f32_16x16x32_bf16 v[196:199], v[144:147], v[96:99], v[180:183]\n\t"
      "v_mfma_f32_16x16x32_bf16 v[196:199], v[148:151], v[100:103], v[196:199]\n\t"
      "v_mfma_f32_16x16x32_bf16 v[196:199], v[152:155], v[104:107], v[196:199]\n\t"
      "v_mfma_f32_16x16x32_bf16 v[196:199], v[156:159], v[108:111], v[196:199]\n\t"
      "s_waitcnt vmcnt(8)\n\t"
      "ds_read_b128 v[112:115], v254 offset:16384\n\t"
      "ds_read_b128 v[116:119], v254 offset:16896\n\t"
      "ds_read_b128 v[120:123], v254 offset:17408\n\t"
      "ds_read_b128 v[124:127], v254 offset:17920\n\t"
      "v_max3_f32 v244, v192, v193, v194\n\t"
      "s_nop 1\n\t"
      "v_max3_f32 v253, v195, v196, v197\n\t"
      "v_max3_f32 v244, v244, v198, v199\n\t"
      "v_max_f32_e32 v244, v244, v253\n\t"
      "v_mov_b32_e32 v253, v244\n\t"
      "s_nop 1\n\t"
      "v_permlane16_swap_b32_e32 v244, v253\n\t"
      "s_nop 0\n\t"
      "v_max_f32_e32 v244, v244, v253\n\t"
      "v_mov_b32_e32 v253, v244\n\t"
      "s_nop 1\n\t"
      "v_permlane32_swap_b32_e32 v244, v253\n\t"
      "s_nop 0\n\t"
      "v_max_f32_e32 v244, v244, v253\n\t"
      "v_mul_f32_e64 v244, -v244, s36\n\t"
      "s_waitcnt lgkmcnt(0)\n\t"
      "v_mfma_f32_16x16x32_bf16 v[200:203], v[160:163], v[112:115], v[176:179]\n\t"
      "v_fma_f32 v192, v192, s36, v244\n\t"
      "v_fma_f32 v194, v194, s36, v244\n\t"
      "v_fma_f32 v193, v193, s36, v244\n\t"
      "v_mfma_f32_16x16x32_bf16 v[200:203], v[164:167], v[116:119], v[200:203]\n\t"
      "v_fma_f32 v195, v195, s36, v244\n\t"
      "v_exp_f32_e32 v195, v195\n\t"
      "v_exp_f32_e32 v193, v193\n\t"
      "v_mfma_f32_16x16x32_bf16 v[200:203], v[168:171], v[120:123], v[200:203]\n\t"
      "v_exp_f32_e32 v194, v194\n\t"
      "v_fma_f32 v198, v198, s36, v244\n\t"
      "v_fma_f32 v199, v199, s36, v244\n\t"
      "v_mfma_f32_16x16x32_bf16 v[200:203], v[172:175], v[124:127], v[200:203]\n\t"
      "v_exp_f32_e32 v198, v198\n\t"
      "v_fma_f32 v196, v196, s36, v244\n\t"
      "v_exp_f32_e32 v196, v196\n\t"
      "v_mfma_f32_16x16x32_bf16 v[204:207], v[144:147], v[112:115], v[180:183]\n\t"
      "v_exp_f32_e32 v192, v192\n\t"
      "v_fma_f32 v197, v197, s36, v244\n\t"
      "v_mfma_f32_16x16x32_bf16 v[204:207], v[148:151], v[116:119], v[204:207]\n\t"
      "v_exp_f32_e32 v197, v197\n\t"
      "v_cvt_pk_bf16_f32 v217, v194, v195\n\t"
      "v_mfma_f32_16x16x32_bf16 v[204:207], v[152:155], v[120:123], v[204:207]\n\t"
      "v_cvt_pk_bf16_f32 v216, v192, v193\n\t"
      "v_cvt_pk_bf16_f32 v218, v196, v197\n\t"
      "v_mfma_f32_16x16x32_bf16 v[204:207], v[156:159], v[124:127], v[204:207]\n\t"
      "v_exp_f32_e32 v199, v199\n\t"
      "s_nop 0\n\t"
      "v_cvt_pk_bf16_f32 v219, v198, v199\n\t"
      "s_waitcnt vmcnt(4)\n\t"
      "s_add_u32 m0, s42, 49152\n\t"
      "s_nop 0\n\t"
      "buffer_load_dwordx4 v251, s[12:15], s31 offen lds\n\t"
      "s_addk_i32 m0, 896\n\t"
      "s_nop 0\n\t"
      "buffer_load_dwordx4 v251, s[12:15], s31 offen offset:128 lds\n\t"
      "s_addk_i32 m0, 7296\n\t"
      "s_nop 0\n\t"
      "buffer_load_dwordx4 v252, s[16:19], s41 offen lds\n\t"
      "s_addk_i32 m0, 896\n\t"
      "s_nop 0\n\t"
      "buffer_load_dwordx4 v252, s[16:19], s41 offen offset:128 lds\n\t"
      "s_mov_b32 s41, s31\n\t"
      "s_add_u32 s31, s31, 0x20000\n\t"
      "s_cmp_lt_u32 s42, s40\n\t"
      "s_cselect_b32 s34, s35, s58\n\t"
      "s_mov_b32 m0, s42\n\t"
      "s_add_u32 s42, s42, s34\n\t"
      "v_add_u32_e32 v248, s34, v248\n\t"
      "v_add_u32_e32 v249, s34, v249\n\t"
      "v_add_u32_e32 v250, s34, v250\n\t"
      "v_max3_f32 v245, v200, v201, v202\n\t"
      "v_max3_f32 v253, v203, v204, v205\n\t"
      "v_max3_f32 v245, v245, v206, v207\n\t"
      "v_max_f32_e32 v245, v245, v253\n\t"
      "v_mov_b32_e32 v253, v245\n\t"
      "s_nop 1\n\t"
      "v_permlane16_swap_b32_e32 v245, v253\n\t"
      "s_nop 0\n\t"
      "v_max_f32_e32 v245, v245, v253\n\t"
      "v_mov_b32_e32 v253, v245\n\t"
      "s_nop 1\n\t"
      "v_permlane32_swap_b32_e32 v245, v253\n\t"
      "s_nop 0\n\t"
      "v_max_f32_e32 v245, v245, v253\n\t"
      "v_mul_f32_e64 v245, -v245, s36\n\t"
      "v_fma_f32 v205, v205, s36, v245\n\t"
      "v_fma_f32 v204, v204, s36, v245\n\t"
      "v_exp_f32_e32 v204, v204\n\t"
      "v_exp_f32_e32 v205, v205\n\t"
      "v_fma_f32 v201, v201, s36, v245\n\t"
      "v_fma_f32 v202, v202, s36, v245\n\t"
      "v_fma_f32 v200, v200, s36, v245\n\t"
      "v_fma_f32 v207, v207, s36, v245\n\t"
      "v_fma_f32 v203, v203, s36, v245\n\t"
      "v_exp_f32_e32 v202, v202\n\t"
      "v_exp_f32_e32 v203, v203\n\t"
      "v_fma_f32 v206, v206, s36, v245\n\t"
      "v_exp_f32_e32 v201, v201\n\t"
      "s_cmp_lg_u32 s30, 1\n\t"
      "s_cbranch_scc1 L_pw_B2_%=_0\n\t"
      "s_waitcnt vmcnt(0)\n\t"
      "L_pw_B2_%=_0:\n\t"
      "s_waitcnt vmcnt(8)\n\t"
      "s_barrier\n\t"
      "ds_read_b128 v[160:163], v248 offset:0\n\t"
      "ds_read_b128 v[164:167], v248 offset:512\n\t"
      "ds_read_b128 v[168:171], v248 offset:1024\n\t"
      "ds_read_b128 v[172:175], v248 offset:1536\n\t"
      "ds_read_b128 v[144:147], v248 offset:4096\n\t"
      "ds_read_b128 v[148:151], v248 offset:4608\n\t"
      "ds_read_b128 v[152:155], v248 offset:5120\n\t"
      "ds_read_b128 v[156:159], v248 offset:5632\n\t"
      "s_cmp_lt_u32 s30, 4\n\t"
      "s_cbranch_scc1 L_pnd_B2_%=\n\t"
      "s_nop 0\n\t"
      "buffer_load_dwordx4 v251, s[12:15], s31 offen lds\n\t"
      "s_addk_i32 m0, 896\n\t"
      "s_nop 0\n\t"
      "buffer_load_dwordx4 v251, s[12:15], s31 offen offset:128 lds\n\t"
      "s_addk_i32 m0, 7296\n\t"
      "s_nop 0\n\t"
      "buffer_load_dwordx4 v252, s[16:19], s41 offen lds\n\t"
      "s_addk_i32 m0, 896\n\t"
      "s_nop 0\n\t"
      "buffer_load_dwordx4 v252, s[16:19], s41 offen offset:128 lds\n\t"
      "s_mov_b32 s41, s31\n\t"
      "s_add_u32 s31, s31, 0x20000\n\t"
      "L_pnd_B2_%=:\n\t"
      "s_sub_u32 s30, s30, 1\n\t"
      "s_cmp_lt_u32 s30, 4\n\t"
      "s_waitcnt lgkmcnt(4)\n\t"
      "s_cbranch_scc1 L_s1_B2_%=\n\t"
      "L_dma_B2_%=:\n\t"
      "v_mfma_f32_16x16x32_bf16 v[192:195], v[160:163], v[96:99], 0\n\t"
      "s_cmp_lt_u32 s42, s40\n\t"
      "ds_read_b64_tr_b16 v[176:177], v249 offset:0\n\t"
      "v_cvt_pk_bf16_f32 v222, v204, v205\n\t"
      "v_cvt_pk_bf16_f32 v221, v202, v203\n\t"
      "v_mfma_f32_16x16x32_bf16 v[192:195], v[164:167], v[100:103], v[192:195]\n\t"
      "s_cselect_b32 s34, s35, s58\n\t"
      "ds_read_b64_tr_b16 v[178:179], v249 offset:4096\n\t"
      "v_add_u32_e32 v248, s34, v248\n\t"
      "v_mfma_f32_16x16x32_bf16 v[192:195], v[168:171], v[104:107], v[192:195]\n\t"
      "s_mov_b32 m0, s42\n\t"
      "v_exp_f32_e32 v206, v206\n\t"
      "v_mfma_f32_16x16x32_bf16 v[192:195], v[172:175], v[108:111], v[192:195]\n\t"
      "s_waitcnt lgkmcnt(2)\n\t"
      "ds_read_b64_tr_b16 v[180:181], v250 offset:0\n\t"
      "v_exp_f32_e32 v207, v207\n\t"
      "v_mfma_f32_16x16x32_bf16 v[196:199], v[144:147], v[96:99], 0\n\t"
      "s_add_u32 s42, s42, s34\n\t"
      "ds_read_b64_tr_b16 v[182:183], v250 offset:4096\n\t"
      "v_cvt_pk_bf16_f32 v223, v206, v207\n\t"
      "v_mfma_f32_16x16x32_bf16 v[196:199], v[148:151], v[100:103], v[196:199]\n\t"
      "ds_read_b64_tr_b16 v[184:185], v249 offset:512\n\t"
      "v_exp_f32_e32 v200, v200\n\t"
      "v_mfma_f32_16x16x32_bf16 v[196:199], v[152:155], v[104:107], v[196:199]\n\t"
      "ds_read_b64_tr_b16 v[186:187], v249 offset:4608\n\t"
      "v_cvt_pk_bf16_f32 v220, v200, v201\n\t"
      "v_mfma_f32_16x16x32_bf16 v[196:199], v[156:159], v[108:111], v[196:199]\n\t"
      "ds_read_b64_tr_b16 v[188:189], v250 offset:512\n\t"
      "v_fma_f32 v192, v192, s36, v244\n\t"
      "v_mfma_f32_16x16x32_bf16 v[204:207], v[144:147], v[112:115], 0\n\t"
      "ds_read_b64_tr_b16 v[190:191], v250 offset:4608\n\t"
      "v_fma_f32 v194, v194, s36, v244\n\t"
      "v_mfma_f32_16x16x32_bf16 v[204:207], v[148:151], v[116:119], v[204:207]\n\t"
      "ds_read_b64_tr_b16 v[144:145], v249 offset:1024\n\t"
      "v_fma_f32 v193, v193, s36, v244\n\t"
      "v_fma_f32 v195, v195, s36, v244\n\t"
      "v_mfma_f32_16x16x32_bf16 v[204:207], v[152:155], v[120:123], v[204:207]\n\t"
      "ds_read_b64_tr_b16 v[146:147], v249 offset:5120\n\t"
      "v_exp_f32_e32 v195, v195\n\t"
      "v_mfma_f32_16x16x32_bf16 v[204:207], v[156:159], v[124:127], v[204:207]\n\t"
      "ds_read_b64_tr_b16 v[148:149], v250 offset:1024\n\t"
      "v_exp_f32_e32 v193, v193\n\t"
      "v_mfma_f32_16x16x32_bf16 v[200:203], v[160:163], v[112:115], 0\n\t"
      "ds_read_b64_tr_b16 v[150:151], v250 offset:5120\n\t"
      "v_exp_f32_e32 v194, v194\n\t"
      "v_mfma_f32_16x16x32_bf16 v[200:203], v[164:167], v[116:119], v[200:203]\n\t"
      "ds_read_b64_tr_b16 v[152:153], v249 offset:1536\n\t"
      "v_fma_f32 v198, v198, s36, v244\n\t"
      "v_mfma_f32_16x16x32_bf16 v[200:203], v[168:171], v[120:123], v[200:203]\n\t"
      "ds_read_b64_tr_b16 v[154:155], v249 offset:5632\n\t"
      "v_fma_f32 v199, v199, s36, v244\n\t"
      "v_mfma_f32_16x16x32_bf16 v[200:203], v[172:175], v[124:127], v[200:203]\n\t"
      "ds_read_b64_tr_b16 v[156:157], v250 offset:1536\n\t"
      "v_exp_f32_e32 v198, v198\n\t"
      "v_mfma_f32_16x16x32_bf16 v[228:231], v[240:243], v[216:219], v[228:231]\n\t"
      "s_waitcnt lgkmcnt(7)\n\t"
      "ds_read_b64_tr_b16 v[158:159], v250 offset:5632\n\t"
      "v_add_u32_e32 v250, s34, v250\n\t"
      "v_fma_f32 v196, v196, s36, v244\n\t"
      "v_mfma_f32_16x16x32_bf16 v[0:3], v[176:179], v[216:219], v[0:3]\n\t"
      "v_exp_f32_e32 v196, v196\n\t"
      "v_fma_f32 v205, v205, s36, v245\n\t"
      "v_mfma_f32_16x16x32_bf16 v[4:7], v[180:183], v[216:219], v[4:7]\n\t"
      "v_exp_f32_e32 v192, v192\n\t"
      "v_fma_f32 v204, v204, s36, v245\n\t"
      "v_mfma_f32_16x16x32_bf16 v[8:11], v[184:187], v[216:219], v[8:11]\n\t"
      "v_fma_f32 v197, v197, s36, v244\n\t"
      "v_exp_f32_e32 v197, v197\n\t"
      "v_mfma_f32_16x16x32_bf16 v[12:15], v[188:191], v[216:219], v[12:15]\n\t"
      "s_waitcnt lgkmcnt(4)\n\t"
      "v_exp_f32_e32 v204, v204\n\t"
      "v_add_u32_e32 v249, s34, v249\n\t"
      "v_mfma_f32_16x16x32_bf16 v[16:19], v[144:147], v[216:219], v[16:19]\n\t"
      "v_exp_f32_e32 v205, v205\n\t"
      "v_fma_f32 v201, v201, s36, v245\n\t"
      "v_mfma_f32_16x16x32_bf16 v[20:23], v[148:151], v[216:219], v[20:23]\n\t"
      "s_waitcnt vmcnt(8) lgkmcnt(0)\n\t"
      "s_barrier\n\t"
      "v_mfma_f32_16x16x32_bf16 v[24:27], v[152:155], v[216:219], v[24:27]\n\t"
      "s_sub_u32 s30, s30, 1\n\t"
      "buffer_load_dwordx4 v251, s[12:15], s31 offen lds\n\t"
      "v_fma_f32 v202, v202, s36, v245\n\t"
      "v_fma_f32 v200, v200, s36, v245\n\t"
      "v_mfma_f32_16x16x32_bf16 v[28:31], v[156:159], v[216:219], v[28:31]\n\t"
      "s_addk_i32 m0, 896\n\t"
      "ds_read_b128 v[160:163], v248 offset:0\n\t"
      "v_cvt_pk_bf16_f32 v217, v194, v195\n\t"
      "v_cvt_pk_bf16_f32 v216, v192, v193\n\t"
      "v_mfma_f32_16x16x32_bf16 v[48:51], v[144:147], v[220:223], v[48:51]\n\t"
      "buffer_load_dwordx4 v251, s[12:15], s31 offen offset:128 lds\n\t"
      "v_cvt_pk_bf16_f32 v218, v196, v197\n\t"
      "v_fma_f32 v207, v207, s36, v245\n\t"
      "v_mfma_f32_16x16x32_bf16 v[52:55], v[148:151], v[220:223], v[52:55]\n\t"
      "s_addk_i32 m0, 7296\n\t"
      "ds_read_b128 v[164:167], v248 offset:512\n\t"
      "v_exp_f32_e32 v199, v199\n\t"
      "v_mfma_f32_16x16x32_bf16 v[56:59], v[152:155], v[220:223], v[56:59]\n\t"
      "ds_read_b128 v[168:171], v248 offset:1024\n\t"
      "ds_read_b128 v[172:175], v248 offset:1536\n\t"
      "buffer_load_dwordx4 v252, s[16:19], s41 offen lds\n\t"
      "v_mfma_f32_16x16x32_bf16 v[60:63], v[156:159], v[220:223], v[60:63]\n\t"
      "s_addk_i32 m0, 896\n\t"
      "ds_read_b128 v[144:147], v248 offset:4096\n\t"
      "v_cvt_pk_bf16_f32 v219, v198, v199\n\t"
      "v_fma_f32 v203, v203, s36, v245\n\t"
      "v_mfma_f32_16x16x32_bf16 v[32:35], v[176:179], v[220:223], v[32:35]\n\t"
      "buffer_load_dwordx4 v252, s[16:19], s41 offen offset:128 lds\n\t"
      "v_exp_f32_e32 v202, v202\n\t"
      "v_mfma_f32_16x16x32_bf16 v[36:39], v[180:183], v[220:223], v[36:39]\n\t"
      "s_mov_b32 s41, s31\n\t"
      "ds_read_b128 v[148:151], v248 offset:4608\n\t"
      "v_exp_f32_e32 v203, v203\n\t"
      "v_mfma_f32_16x16x32_bf16 v[40:43], v[184:187], v[220:223], v[40:43]\n\t"
      "s_add_u32 s31, s31, 0x20000\n\t"
      "ds_read_b128 v[152:155], v248 offset:5120\n\t"
      "ds_read_b128 v[156:159], v248 offset:5632\n\t"
      "v_fma_f32 v206, v206, s36, v245\n\t"
      "v_mfma_f32_16x16x32_bf16 v[44:47], v[188:191], v[220:223], v[44:47]\n\t"
      "s_cmp_ge_u32 s30, s59\n\t"
      "v_exp_f32_e32 v201, v201\n\t"
      "v_mfma_f32_16x16x32_bf16 v[232:235], v[240:243], v[220:223], v[232:235]\n\t"
      "s_waitcnt lgkmcnt(4)\n\t"
      "s_cbranch_scc1 L_dma_B2_%=\n\t"
      "L_s1_B2_%=:\n\t"
      "v_mbcnt_lo_u32_b32 v252, -1, 0\n\t"
      "v_mbcnt_hi_u32_b32 v252, -1, v252\n\t"
      "v_and_b32_e32 v253, 7, v252\n\t"
      "v_bfe_u32 v251, v252, 3, 1\n\t"
      "v_lshl_or_b32 v253, v253, 1, v251\n\t"
      "v_lshrrev_b32_e32 v251, 4, v252\n\t"
      "v_xor_b32_e32 v253, v253, v251\n\t"
      "v_and_b32_e32 v251, 15, v252\n\t"
      "v_lshlrev_b32_e32 v251, 4, v251\n\t"
      "v_add_lshl_u32 v253, v253, v251, 3\n\t"
      "v_lshrrev_b32_e32 v251, 3, v252\n\t"
      "s_movk_i32 s37, 0xf8\n\t"
      "v_mad_u32_u24 v254, v251, s37, v252\n\t"
      "v_xor_b32_e32 v251, v251, v252\n\t"
      "v_lshlrev_b32_e32 v251, 4, v251\n\t"
      "v_lshlrev_b32_e32 v252, 4, v254\n\t"
      "s_cmp_gt_u32 s94, 8\n\t"
      "s_cbranch_scc1 L_kp_B2_%=\n\t"
      "s_setprio 0\n\t"
      "L_kp_B2_%=:\n\t"
      "s_cmp_eq_u32 s30, 0\n\t"
      "s_cbranch_scc1 L_dr_B2_%=\n\t"
      "L_nod_B2_%=:\n\t"
      "v_mfma_f32_16x16x32_bf16 v[192:195], v[160:163], v[96:99], 0\n\t"
      "s_cmp_lt_u32 s42, s40\n\t"
      "ds_read_b64_tr_b16 v[176:177], v249 offset:0\n\t"
      "v_cvt_pk_bf16_f32 v222, v204, v205\n\t"
      "v_cvt_pk_bf16_f32 v221, v202, v203\n\t"
      "v_mfma_f32_16x16x32_bf16 v[192:195], v[164:167], v[100:103], v[192:195]\n\t"
      "s_cselect_b32 s34, s35, s58\n\t"
      "ds_read_b64_tr_b16 v[178:179], v249 offset:4096\n\t"
      "v_add_u32_e32 v248, s34, v248\n\t"
      "v_mfma_f32_16x16x32_bf16 v[192:195], v[168:171], v[104:107], v[192:195]\n\t"
      "s_mov_b32 m0, s42\n\t"
      "v_exp_f32_e32 v206, v206\n\t"
      "v_mfma_f32_16x16x32_bf16 v[192:195], v[172:175], v[108:111], v[192:195]\n\t"
      "s_waitcnt lgkmcnt(2)\n\t"
      "ds_read_b64_tr_b16 v[180:181], v250 offset:0\n\t"
      "v_exp_f32_e32 v207, v207\n\t"
      "v_mfma_f32_16x16x32_bf16 v[196:199], v[144:147], v[96:99], 0\n\t"
      "s_add_u32 s42, s42, s34\n\t"
      "ds_read_b64_tr_b16 v[182:183], v250 offset:4096\n\t"
      "v_cvt_pk_bf16_f32 v223, v206, v207\n\t"
      "v_mfma_f32_16x16x32_bf16 v[196:199], v[148:151], v[100:103], v[196:199]\n\t"
      "ds_read_b64_tr_b16 v[184:185], v249 offset:512\n\t"
      "v_exp_f32_e32 v200, v200\n\t"
      "v_mfma_f32_16x16x32_bf16 v[196:199], v[152:155], v[104:107], v[196:199]\n\t"
      "ds_read_b64_tr_b16 v[186:187], v249 offset:4608\n\t"
      "v_cvt_pk_bf16_f32 v220, v200, v201\n\t"
      "v_mfma_f32_16x16x32_bf16 v[196:199], v[156:159], v[108:111], v[196:199]\n\t"
      "ds_read_b64_tr_b16 v[188:189], v250 offset:512\n\t"
      "v_fma_f32 v192, v192, s36, v244\n\t"
      "v_mfma_f32_16x16x32_bf16 v[204:207], v[144:147], v[112:115], 0\n\t"
      "ds_read_b64_tr_b16 v[190:191], v250 offset:4608\n\t"
      "v_fma_f32 v194, v194, s36, v244\n\t"
      "v_mfma_f32_16x16x32_bf16 v[204:207], v[148:151], v[116:119], v[204:207]\n\t"
      "ds_read_b64_tr_b16 v[144:145], v249 offset:1024\n\t"
      "v_fma_f32 v193, v193, s36, v244\n\t"
      "v_fma_f32 v195, v195, s36, v244\n\t"
      "v_mfma_f32_16x16x32_bf16 v[204:207], v[152:155], v[120:123], v[204:207]\n\t"
      "ds_read_b64_tr_b16 v[146:147], v249 offset:5120\n\t"
      "v_exp_f32_e32 v195, v195\n\t"
      "v_mfma_f32_16x16x32_bf16 v[204:207], v[156:159], v[124:127], v[204:207]\n\t"
      "ds_read_b64_tr_b16 v[148:149], v250 offset:1024\n\t"
      "v_exp_f32_e32 v193, v193\n\t"
      "v_mfma_f32_16x16x32_bf16 v[200:203], v[160:163], v[112:115], 0\n\t"
      "ds_read_b64_tr_b16 v[150:151], v250 offset:5120\n\t"
      "v_exp_f32_e32 v194, v194\n\t"
      "v_mfma_f32_16x16x32_bf16 v[200:203], v[164:167], v[116:119], v[200:203]\n\t"
      "ds_read_b64_tr_b16 v[152:153], v249 offset:1536\n\t"
      "v_fma_f32 v198, v198, s36, v244\n\t"
      "v_mfma_f32_16x16x32_bf16 v[200:203], v[168:171], v[120:123], v[200:203]\n\t"
      "ds_read_b64_tr_b16 v[154:155], v249 offset:5632\n\t"
      "v_fma_f32 v199, v199, s36, v244\n\t"
      "v_mfma_f32_16x16x32_bf16 v[200:203], v[172:175], v[124:127], v[200:203]\n\t"
      "ds_read_b64_tr_b16 v[156:157], v250 offset:1536\n\t"
      "v_exp_f32_e32 v198, v198\n\t"
      "v_mfma_f32_16x16x32_bf16 v[228:231], v[240:243], v[216:219], v[228:231]\n\t"
      "s_waitcnt lgkmcnt(7)\n\t"
      "ds_read_b64_tr_b16 v[158:159], v250 offset:5632\n\t"
      "v_add_u32_e32 v250, s34, v250\n\t"
      "v_fma_f32 v196, v196, s36, v244\n\t"
      "v_mfma_f32_16x16x32_bf16 v[0:3], v[176:179], v[216:219], v[0:3]\n\t"
      "v_exp_f32_e32 v196, v196\n\t"
      "v_fma_f32 v205, v205, s36, v245\n\t"
      "v_mfma_f32_16x16x32_bf16 v[4:7], v[180:183], v[216:219], v[4:7]\n\t"
      "v_exp_f32_e32 v192, v192\n\t"
      "v_fma_f32 v204, v204, s36, v245\n\t"
      "v_mfma_f32_16x16x32_bf16 v[8:11], v[184:187], v[216:219], v[8:11]\n\t"
      "v_fma_f32 v197, v197, s36, v244\n\t"
      "v_exp_f32_e32 v197, v197\n\t"
      "v_mfma_f32_16x16x32_bf16 v[12:15], v[188:191], v[216:219], v[12:15]\n\t"
      "s_waitcnt lgkmcnt(4)\n\t"
      "v_exp_f32_e32 v204, v204\n\t"
      "v_add_u32_e32 v249, s34, v249\n\t"
      "v_mfma_f32_16x16x32_bf16 v[16:19], v[144:147], v[216:219], v[16:19]\n\t"
      "v_exp_f32_e32 v205, v205\n\t"
      "v_fma_f32 v201, v201, s36, v245\n\t"
      "v_mfma_f32_16x16x32_bf16 v[20:23], v[148:151], v[216:219], v[20:23]\n\t"
      "s_waitcnt vmcnt(0) lgkmcnt(0)\n\t"
      "s_barrier\n\t"
      "v_mfma_f32_16x16x32_bf16 v[24:27], v[152:155], v[216:219], v[24:27]\n\t"
      "s_sub_u32 s30, s30, 1\n\t"
      "v_fma_f32 v202, v202, s36, v245\n\t"
      "v_fma_f32 v200, v200, s36, v245\n\t"
      "v_mfma_f32_16x16x32_bf16 v[28:31], v[156:159], v[216:219], v[28:31]\n\t"
      "ds_read_b128 v[160:163], v248 offset:0\n\t"
      "v_cvt_pk_bf16_f32 v217, v194, v195\n\t"
      "v_cvt_pk_bf16_f32 v216, v192, v193\n\t"
      "v_mfma_f32_16x16x32_bf16 v[48:51], v[144:147], v[220:223], v[48:51]\n\t"
      "v_cvt_pk_bf16_f32 v218, v196, v197\n\t"
      "v_fma_f32 v207, v207, s36, v245\n\t"
      "v_mfma_f32_16x16x32_bf16 v[52:55], v[148:151], v[220:223], v[52:55]\n\t"
      "ds_read_b128 v[164:167], v248 offset:512\n\t"
      "v_exp_f32_e32 v199, v199\n\t"
      "v_mfma_f32_16x16x32_bf16 v[56:59], v[152:155], v[220:223], v[56:59]\n\t"
      "ds_read_b128 v[168:171], v248 offset:1024\n\t"
      "ds_read_b128 v[172:175], v248 offset:1536\n\t"
      "v_mfma_f32_16x16x32_bf16 v[60:63], v[156:159], v[220:223], v[60:63]\n\t"
      "ds_read_b128 v[144:147], v248 offset:4096\n\t"
      "v_cvt_pk_bf16_f32 v219, v198, v199\n\t"
      "v_fma_f32 v203, v203, s36, v245\n\t"
      "v_mfma_f32_16x16x32_bf16 v[32:35], v[176:179], v[220:223], v[32:35]\n\t"
      "v_exp_f32_e32 v202, v202\n\t"
      "v_mfma_f32_16x16x32_bf16 v[36:39], v[180:183], v[220:223], v[36:39]\n\t"
      "ds_read_b128 v[148:151], v248 offset:4608\n\t"
      "v_exp_f32_e32 v203, v203\n\t"
      "v_mfma_f32_16x16x32_bf16 v[40:43], v[184:187], v[220:223], v[40:43]\n\t"
      "ds_read_b128 v[152:155], v248 offset:5120\n\t"
      "ds_read_b128 v[156:159], v248 offset:5632\n\t"
      "v_fma_f32 v206, v206, s36, v245\n\t"
      "v_mfma_f32_16x16x32_bf16 v[44:47], v[188:191], v[220:223], v[44:47]\n\t"
      "s_cmp_ge_u32 s30, 1\n\t"
      "v_exp_f32_e32 v201, v201\n\t"
      "v_mfma_f32_16x16x32_bf16 v[232:235], v[240:243], v[220:223], v[232:235]\n\t"
      "s_waitcnt lgkmcnt(4)\n\t"
      "s_cbranch_scc1 L_nod_B2_%=\n\t"
      "L_dr_B2_%=:\n\t"
      "v_mfma_f32_16x16x32_bf16 v[228:231], v[240:243], v[216:219], v[228:231]\n\t"
      "ds_read_b64_tr_b16 v[176:177], v249 offset:0\n\t"
      "ds_read_b64_tr_b16 v[178:179], v249 offset:4096\n\t"
      "ds_read_b64_tr_b16 v[180:181], v250 offset:0\n\t"
      "ds_read_b64_tr_b16 v[182:183], v250 offset:4096\n\t"
      "ds_read_b64_tr_b16 v[184:185], v249 offset:512\n\t"
      "ds_read_b64_tr_b16 v[186:187], v249 offset:4608\n\t"
      "ds_read_b64_tr_b16 v[188:189], v250 offset:512\n\t"
      "ds_read_b64_tr_b16 v[190:191], v250 offset:4608\n\t"
      "ds_read_b64_tr_b16 v[144:145], v249 offset:1024\n\t"
      "ds_read_b64_tr_b16 v[146:147], v249 offset:5120\n\t"
      "ds_read_b64_tr_b16 v[148:149], v250 offset:1024\n\t"
      "v_exp_f32_e32 v206, v206\n\t"
      "v_exp_f32_e32 v207, v207\n\t"
      "v_exp_f32_e32 v200, v200\n\t"
      "v_cvt_pk_bf16_f32 v222, v204, v205\n\t"
      "v_cvt_pk_bf16_f32 v221, v202, v203\n\t"
      "v_cvt_pk_bf16_f32 v223, v206, v207\n\t"
      "v_cvt_pk_bf16_f32 v220, v200, v201\n\t"
      "ds_read_b64_tr_b16 v[150:151], v250 offset:5120\n\t"
      "s_nop 0\n\t"
      "v_mfma_f32_16x16x32_bf16 v[232:235], v[240:243], v[220:223], v[232:235]\n\t"
      "ds_read_b64_tr_b16 v[152:153], v249 offset:1536\n\t"
      "ds_read_b64_tr_b16 v[154:155], v249 offset:5632\n\t"
      "ds_read_b64_tr_b16 v[156:157], v250 offset:1536\n\t"
      "ds_read_b64_tr_b16 v[158:159], v250 offset:5632\n\t"
      "s_waitcnt lgkmcnt(12)\n\t"
      "v_mfma_f32_16x16x32_bf16 v[0:3], v[176:179], v[216:219], v[0:3]\n\t"
      "s_lshr_b32 s37, s42, 14\n\t"
      "v_rcp_f32_e32 v230, v228\n\t"
      "v_mfma_f32_16x16x32_bf16 v[4:7], v[180:183], v[216:219], v[4:7]\n\t"
      "s_add_u32 s37, s37, s26\n\t"
      "s_add_u32 s37, s37, 1\n\t"
      "s_sub_u32 s38, s37, 5\n\t"
      "s_cselect_b32 s37, s37, s38\n\t"
      "s_waitcnt lgkmcnt(8)\n\t"
      "v_mfma_f32_16x16x32_bf16 v[8:11], v[184:187], v[216:219], v[8:11]\n\t"
      "s_lshl_b32 s42, s37, 14\n\t"
      "v_add_u32_e32 v253, s42, v253\n\t"
      "v_mul_f32_e32 v0, v230, v0\n\t"
      "v_mul_f32_e32 v1, v230, v1\n\t"
      "v_mfma_f32_16x16x32_bf16 v[12:15], v[188:191], v[216:219], v[12:15]\n\t"
      "v_mul_f32_e32 v2, v230, v2\n\t"
      "v_mul_f32_e32 v3, v230, v3\n\t"
      "s_waitcnt lgkmcnt(4)\n\t"
      "v_mfma_f32_16x16x32_bf16 v[16:19], v[144:147], v[216:219], v[16:19]\n\t"
      "v_mul_f32_e32 v4, v230, v4\n\t"
      "v_mul_f32_e32 v5, v230, v5\n\t"
      "v_mfma_f32_16x16x32_bf16 v[20:23], v[148:151], v[216:219], v[20:23]\n\t"
      "v_cvt_pk_bf16_f32 v0, v0, v1\n\t"
      "v_cvt_pk_bf16_f32 v1, v2, v3\n\t"
      "s_waitcnt lgkmcnt(0)\n\t"
      "v_mfma_f32_16x16x32_bf16 v[24:27], v[152:155], v[216:219], v[24:27]\n\t"
      "v_mul_f32_e32 v6, v230, v6\n\t"
      "v_mul_f32_e32 v7, v230, v7\n\t"
      "v_mfma_f32_16x16x32_bf16 v[60:63], v[156:159], v[220:223], v[60:63]\n\t"
      "v_mul_f32_e32 v8, v230, v8\n\t"
      "v_mul_f32_e32 v9, v230, v9\n\t"
      "v_mfma_f32_16x16x32_bf16 v[28:31], v[156:159], v[216:219], v[28:31]\n\t"
      "v_cvt_pk_bf16_f32 v4, v4, v5\n\t"
      "v_cvt_pk_bf16_f32 v5, v6, v7\n\t"
      "v_mfma_f32_16x16x32_bf16 v[32:35], v[176:179], v[220:223], v[32:35]\n\t"
      "v_mul_f32_e32 v10, v230, v10\n\t"
      "v_mul_f32_e32 v11, v230, v11\n\t"
      "v_mfma_f32_16x16x32_bf16 v[36:39], v[180:183], v[220:223], v[36:39]\n\t"
      "v_mul_f32_e32 v12, v230, v12\n\t"
      "v_mul_f32_e32 v13, v230, v13\n\t"
      "v_mfma_f32_16x16x32_bf16 v[40:43], v[184:187], v[220:223], v[40:43]\n\t"
      "v_cvt_pk_bf16_f32 v8, v8, v9\n\t"
      "v_cvt_pk_bf16_f32 v9, v10, v11\n\t"
      "v_mfma_f32_16x16x32_bf16 v[44:47], v[188:191], v[220:223], v[44:47]\n\t"
      "v_mul_f32_e32 v14, v230, v14\n\t"
      "v_mul_f32_e32 v15, v230, v15\n\t"
      "v_mfma_f32_16x16x32_bf16 v[48:51], v[144:147], v[220:223], v[48:51]\n\t"
      "v_cvt_pk_bf16_f32 v12, v12, v13\n\t"
      "v_cvt_pk_bf16_f32 v13, v14, v15\n\t"
      "v_mfma_f32_16x16x32_bf16 v[52:55], v[148:151], v[220:223], v[52:55]\n\t"
      "v_add_u32_e32 v251, s42, v251\n\t"
      "v_xor_b32_e32 v248, 32, v253\n\t"
      "v_mfma_f32_16x16x32_bf16 v[56:59], v[152:155], v[220:223], v[56:59]\n\t"
      "v_xor_b32_e32 v254, 64, v253\n\t"
      "v_xor_b32_e32 v143, 96, v253\n\t"
      "ds_write_b64 v253, v[0:1] offset:0\n\t"
      "ds_write_b64 v248, v[4:5] offset:0\n\t"
      "ds_write_b64 v254, v[8:9] offset:0\n\t"
      "ds_write_b64 v143, v[12:13] offset:0\n\t"
      "ds_read_b128 v[96:99], v251 offset:0\n\t"
      "ds_read_b128 v[100:103], v251 offset:1024\n\t"
      "v_rcp_f32_e32 v234, v232\n\t"
      "v_pk_mul_f32 v[16:17], v[16:17], v[230:231] op_sel_hi:[1,0]\n\t"
      "v_add_f32_e32 v229, v228, v232\n\t"
      "v_add_f32_e32 v229, v229, v230\n\t"
      "v_add_f32_e32 v229, v229, v234\n\t"
      "v_cmp_nge_f32_e32 vcc, 0x4f800000, v229\n\t"
      "v_pk_mul_f32 v[18:19], v[18:19], v[230:231] op_sel_hi:[1,0]\n\t"
      "s_mov_b32 s39, 1\n\t"
      "s_cbranch_vccnz L_end_%=\n\t"
      "s_mov_b32 s39, 0\n\t"
      "v_cvt_pk_bf16_f32 v16, v16, v17\n\t"
      "v_cvt_pk_bf16_f32 v17, v18, v19\n\t"
      "v_pk_mul_f32 v[20:21], v[20:21], v[230:231] op_sel_hi:[1,0]\n\t"
      "v_pk_mul_f32 v[22:23], v[22:23], v[230:231] op_sel_hi:[1,0]\n\t"
      "v_cvt_pk_bf16_f32 v20, v20, v21\n\t"
      "v_cvt_pk_bf16_f32 v21, v22, v23\n\t"
      "v_pk_mul_f32 v[24:25], v[24:25], v[230:231] op_sel_hi:[1,0]\n\t"
      "v_pk_mul_f32 v[26:27], v[26:27], v[230:231] op_sel_hi:[1,0]\n\t"
      "v_cvt_pk_bf16_f32 v24, v24, v25\n\t"
      "v_cvt_pk_bf16_f32 v25, v26, v27\n\t"
      "v_pk_mul_f32 v[28:29], v[28:29], v[230:231] op_sel_hi:[1,0]\n\t"
      "s_waitcnt lgkmcnt(1)\n\t"
      "buffer_store_dwordx4 v[96:99], v252, s[20:23], s27 offen nt sc1\n\t"
      "s_add_u32 s38, s27, 0x8000\n\t"
      "s_waitcnt lgkmcnt(0)\n\t"
      "v_mov_b64_e32 v[104:105], v[100:101]\n\t"
      "buffer_store_dwordx4 v[102:105], v252, s[20:23], s38 offen nt sc1\n\t"
      "v_pk_mul_f32 v[30:31], v[30:31], v[230:231] op_sel_hi:[1,0]\n\t"
      "v_cvt_pk_bf16_f32 v28, v28, v29\n\t"
      "v_cvt_pk_bf16_f32 v29, v30, v31\n\t"
      "ds_write_b64 v253, v[16:17] offset:2048\n\t"
      "ds_write_b64 v248, v[20:21] offset:2048\n\t"
      "ds_write_b64 v254, v[24:25] offset:2048\n\t"
      "ds_write_b64 v143, v[28:29] offset:2048\n\t"
      "ds_read_b128 v[106:109], v251 offset:2048\n\t"
      "ds_read_b128 v[110:113], v251 offset:3072\n\t"
      "v_pk_mul_f32 v[32:33], v[32:33], v[234:235] op_sel_hi:[1,0]\n\t"
      "v_pk_mul_f32 v[34:35], v[34:35], v[234:235] op_sel_hi:[1,0]\n\t"
      "v_cvt_pk_bf16_f32 v32, v32, v33\n\t"
      "v_cvt_pk_bf16_f32 v33, v34, v35\n\t"
      "v_pk_mul_f32 v[36:37], v[36:37], v[234:235] op_sel_hi:[1,0]\n\t"
      "v_pk_mul_f32 v[38:39], v[38:39], v[234:235] op_sel_hi:[1,0]\n\t"
      "v_cvt_pk_bf16_f32 v36, v36, v37\n\t"
      "v_cvt_pk_bf16_f32 v37, v38, v39\n\t"
      "v_pk_mul_f32 v[40:41], v[40:41], v[234:235] op_sel_hi:[1,0]\n\t"
      "v_pk_mul_f32 v[42:43], v[42:43], v[234:235] op_sel_hi:[1,0]\n\t"
      "v_cvt_pk_bf16_f32 v40, v40, v41\n\t"
      "v_cvt_pk_bf16_f32 v41, v42, v43\n\t"
      "v_pk_mul_f32 v[44:45], v[44:45], v[234:235] op_sel_hi:[1,0]\n\t"
      "v_pk_mul_f32 v[46:47], v[46:47], v[234:235] op_sel_hi:[1,0]\n\t"
      "v_cvt_pk_bf16_f32 v44, v44, v45\n\t"
      "v_cvt_pk_bf16_f32 v45, v46, v47\n\t"
      "ds_write_b64 v253, v[32:33] offset:4096\n\t"
      "ds_write_b64 v248, v[36:37] offset:4096\n\t"
      "ds_write_b64 v254, v[40:41] offset:4096\n\t"
      "ds_write_b64 v143, v[44:45] offset:4096\n\t"
      "ds_read_b128 v[116:119], v251 offset:4096\n\t"
      "ds_read_b128 v[120:123], v251 offset:5120\n\t"
      "s_waitcnt lgkmcnt(7)\n\t"
      "buffer_store_dwordx4 v[106:109], v252, s[20:23], s27 offen offset:128 nt sc1\n\t"
      "s_waitcnt lgkmcnt(6)\n\t"
      "v_mov_b64_e32 v[114:115], v[110:111]\n\t"
      "buffer_store_dwordx4 v[112:115], v252, s[20:23], s38 offen offset:128 nt sc1\n\t"
      "s_add_u32 s27, s27, 0x10000\n\t"
      "v_pk_mul_f32 v[48:49], v[48:49], v[234:235] op_sel_hi:[1,0]\n\t"
      "v_pk_mul_f32 v[50:51], v[50:51], v[234:235] op_sel_hi:[1,0]\n\t"
      "v_cvt_pk_bf16_f32 v48, v48, v49\n\t"
      "v_cvt_pk_bf16_f32 v49, v50, v51\n\t"
      "v_pk_mul_f32 v[52:53], v[52:53], v[234:235] op_sel_hi:[1,0]\n\t"
      "v_pk_mul_f32 v[54:55], v[54:55], v[234:235] op_sel_hi:[1,0]\n\t"
      "v_cvt_pk_bf16_f32 v52, v52, v53\n\t"
      "v_cvt_pk_bf16_f32 v53, v54, v55\n\t"
      "v_pk_mul_f32 v[56:57], v[56:57], v[234:235] op_sel_hi:[1,0]\n\t"
      "v_pk_mul_f32 v[58:59], v[58:59], v[234:235] op_sel_hi:[1,0]\n\t"
      "v_cvt_pk_bf16_f32 v56, v56, v57\n\t"
      "v_cvt_pk_bf16_f32 v57, v58, v59\n\t"
      "v_pk_mul_f32 v[60:61], v[60:61], v[234:235] op_sel_hi:[1,0]\n\t"
      "v_pk_mul_f32 v[62:63], v[62:63], v[234:235] op_sel_hi:[1,0]\n\t"
      "v_cvt_pk_bf16_f32 v60, v60, v61\n\t"
      "v_cvt_pk_bf16_f32 v61, v62, v63\n\t"
      "ds_write_b64 v253, v[48:49] offset:6144\n\t"
      "ds_write_b64 v248, v[52:53] offset:6144\n\t"
      "ds_write_b64 v254, v[56:57] offset:6144\n\t"
      "ds_write_b64 v143, v[60:61] offset:6144\n\t"
      "s_waitcnt lgkmcnt(5)\n\t"
      "buffer_store_dwordx4 v[116:119], v252, s[20:23], s27 offen nt sc1\n\t"
      "s_add_u32 s38, s27, 0x8000\n\t"
      "s_waitcnt lgkmcnt(4)\n\t"
      "v_mov_b64_e32 v[124:125], v[120:121]\n\t"
      "buffer_store_dwordx4 v[122:125], v252, s[20:23], s38 offen nt sc1\n\t"
      "ds_read_b128 v[126:129], v251 offset:6144\n\t"
      "ds_read_b128 v[130:133], v251 offset:7168\n\t"
      "s_waitcnt lgkmcnt(1)\n\t"
      "buffer_store_dwordx4 v[126:129], v252, s[20:23], s27 offen offset:128 nt sc1\n\t"
      "s_waitcnt lgkmcnt(0)\n\t"
      "v_mov_b64_e32 v[134:135], v[130:131]\n\t"
      "buffer_store_dwordx4 v[132:135], v252, s[20:23], s38 offen offset:128 nt sc1\n\t"
      "s_add_u32 s27, s27, 0x10000\n\t"
      "s_add_u32 s37, s7, 16\n\t"
      "s_cmp_lt_u32 s37, s54\n\t"
      "s_cbranch_scc1 L_end_%=\n\t"
      "s_cmp_lg_u32 s43, 0\n\t"
      "s_cbranch_scc1 L_end_%=\n\t"
      "s_endpgm\n\t"
      "L_B3_%=:\n\t"
      "s_waitcnt vmcnt(16)\n\t"
      "s_barrier\n\t"
      "ds_read_b128 v[160:163], v248 offset:0\n\t"
      "ds_read_b128 v[164:167], v248 offset:512\n\t"
      "ds_read_b128 v[168:171], v248 offset:1024\n\t"
      "ds_read_b128 v[172:175], v248 offset:1536\n\t"
      "ds_read_b128 v[144:147], v248 offset:4096\n\t"
      "ds_read_b128 v[148:151], v248 offset:4608\n\t"
      "ds_read_b128 v[152:155], v248 offset:5120\n\t"
      "ds_read_b128 v[156:159], v248 offset:5632\n\t"
      "s_waitcnt vmcnt(12)\n\t"
      "ds_read_b128 v[96:99], v254 offset:0\n\t"
      "ds_read_b128 v[100:103], v254 offset:512\n\t"
      "ds_read_b128 v[104:107], v254 offset:1024\n\t"
      "ds_read_b128 v[108:111], v254 offset:1536\n\t"
      "s_waitcnt lgkmcnt(0)\n\t"
      "s_add_u32 m0, s42, 32768\n\t"
      "s_nop 0\n\t"
      "buffer_load_dwordx4 v251, s[12:15], s31 offen lds\n\t"
      "s_addk_i32 m0, 896\n\t"
      "s_nop 0\n\t"
      "buffer_load_dwordx4 v251, s[12:15], s31 offen offset:128 lds\n\t"
      "s_addk_i32 m0, 7296\n\t"
      "s_nop 0\n\t"
      "buffer_load_dwordx4 v252, s[16:19], s41 offen lds\n\t"
      "s_addk_i32 m0, 896\n\t"
      "s_nop 0\n\t"
      "buffer_load_dwordx4 v252, s[16:19], s41 offen offset:128 lds\n\t"
      "s_mov_b32 s41, s31\n\t"
      "s_add_u32 s31, s31, 0x20000\n\t"
      "v_mfma_f32_16x16x32_bf16 v[192:195], v[160:163], v[96:99], v[176:179]\n\t"
      "v_mfma_f32_16x16x32_bf16 v[192:195], v[164:167], v[100:103], v[192:195]\n\t"
      "v_mfma_f32_16x16x32_bf16 v[192:195], v[168:171], v[104:107], v[192:195]\n\t"
      "v_mfma_f32_16x16x32_bf16 v[192:195], v[172:175], v[108:111], v[192:195]\n\t"
      "v_mfma_f32_16x16x32_bf16 v[196:199], v[144:147], v[96:99], v[180:183]\n\t"
      "v_mfma_f32_16x16x32_bf16 v[196:199], v[148:151], v[100:103], v[196:199]\n\t"
      "v_mfma_f32_16x16x32_bf16 v[196:199], v[152:155], v[104:107], v[196:199]\n\t"
      "v_mfma_f32_16x16x32_bf16 v[196:199], v[156:159], v[108:111], v[196:199]\n\t"
      "s_waitcnt vmcnt(12)\n\t"
      "ds_read_b128 v[112:115], v254 offset:16384\n\t"
      "ds_read_b128 v[116:119], v254 offset:16896\n\t"
      "ds_read_b128 v[120:123], v254 offset:17408\n\t"
      "ds_read_b128 v[124:127], v254 offset:17920\n\t"
      "v_max3_f32 v244, v192, v193, v194\n\t"
      "s_nop 1\n\t"
      "v_max3_f32 v253, v195, v196, v197\n\t"
      "v_max3_f32 v244, v244, v198, v199\n\t"
      "v_max_f32_e32 v244, v244, v253\n\t"
      "v_mov_b32_e32 v253, v244\n\t"
      "s_nop 1\n\t"
      "v_permlane16_swap_b32_e32 v244, v253\n\t"
      "s_nop 0\n\t"
      "v_max_f32_e32 v244, v244, v253\n\t"
      "v_mov_b32_e32 v253, v244\n\t"
      "s_nop 1\n\t"
      "v_permlane32_swap_b32_e32 v244, v253\n\t"
      "s_nop 0\n\t"
      "v_max_f32_e32 v244, v244, v253\n\t"
      "v_mul_f32_e64 v244, -v244, s36\n\t"
      "s_waitcnt lgkmcnt(0)\n\t"
      "v_mfma_f32_16x16x32_bf16 v[200:203], v[160:163], v[112:115], v[176:179]\n\t"
      "v_fma_f32 v194, v194, s36, v244\n\t"
      "v_fma_f32 v192, v192, s36, v244\n\t"
      "v_fma_f32 v195, v195, s36, v244\n\t"
      "v_mfma_f32_16x16x32_bf16 v[200:203], v[164:167], v[116:119], v[200:203]\n\t"
      "v_fma_f32 v193, v193, s36, v244\n\t"
      "v_exp_f32_e32 v195, v195\n\t"
      "v_exp_f32_e32 v193, v193\n\t"
      "v_mfma_f32_16x16x32_bf16 v[200:203], v[168:171], v[120:123], v[200:203]\n\t"
      "v_fma_f32 v199, v199, s36, v244\n\t"
      "v_fma_f32 v198, v198, s36, v244\n\t"
      "v_fma_f32 v197, v197, s36, v244\n\t"
      "v_mfma_f32_16x16x32_bf16 v[200:203], v[172:175], v[124:127], v[200:203]\n\t"
      "v_fma_f32 v196, v196, s36, v244\n\t"
      "v_exp_f32_e32 v194, v194\n\t"
      "v_exp_f32_e32 v198, v198\n\t"
      "v_mfma_f32_16x16x32_bf16 v[204:207], v[144:147], v[112:115], v[180:183]\n\t"
      "v_exp_f32_e32 v192, v192\n\t"
      "v_exp_f32_e32 v199, v199\n\t"
      "v_mfma_f32_16x16x32_bf16 v[204:207], v[148:151], v[116:119], v[204:207]\n\t"
      "v_exp_f32_e32 v197, v197\n\t"
      "v_cvt_pk_bf16_f32 v216, v192, v193\n\t"
      "v_mfma_f32_16x16x32_bf16 v[204:207], v[152:155], v[120:123], v[204:207]\n\t"
      "v_cvt_pk_bf16_f32 v217, v194, v195\n\t"
      "v_cvt_pk_bf16_f32 v219, v198, v199\n\t"
      "v_mfma_f32_16x16x32_bf16 v[204:207], v[156:159], v[124:127], v[204:207]\n\t"
      "v_exp_f32_e32 v196, v196\n\t"
      "s_nop 0\n\t"
      "v_cvt_pk_bf16_f32 v218, v196, v197\n\t"
      "s_waitcnt vmcnt(8)\n\t"
      "ds_read_b128 v[128:131], v254 offset:32768\n\t"
      "ds_read_b128 v[132:135], v254 offset:33280\n\t"
      "ds_read_b128 v[136:139], v254 offset:33792\n\t"
      "ds_read_b128 v[140:143], v254 offset:34304\n\t"
      "v_max3_f32 v245, v200, v201, v202\n\t"
      "v_max3_f32 v253, v203, v204, v205\n\t"
      "v_max3_f32 v245, v245, v206, v207\n\t"
      "v_max_f32_e32 v245, v245, v253\n\t"
      "v_mov_b32_e32 v253, v245\n\t"
      "s_nop 1\n\t"
      "v_permlane16_swap_b32_e32 v245, v253\n\t"
      "s_nop 0\n\t"
      "v_max_f32_e32 v245, v245, v253\n\t"
      "v_mov_b32_e32 v253, v245\n\t"
      "s_nop 1\n\t"
      "v_permlane32_swap_b32_e32 v245, v253\n\t"
      "s_nop 0\n\t"
      "v_max_f32_e32 v245, v245, v253\n\t"
      "v_mul_f32_e64 v245, -v245, s36\n\t"
      "s_waitcnt lgkmcnt(0)\n\t"
      "v_mfma_f32_16x16x32_bf16 v[208:211], v[160:163], v[128:131], v[176:179]\n\t"
      "v_fma_f32 v202, v202, s36, v245\n\t"
      "v_fma_f32 v200, v200, s36, v245\n\t"
      "v_fma_f32 v201, v201, s36, v245\n\t"
      "v_mfma_f32_16x16x32_bf16 v[208:211], v[164:167], v[132:135], v[208:211]\n\t"
      "v_exp_f32_e32 v201, v201\n\t"
      "v_exp_f32_e32 v202, v202\n\t"
      "v_exp_f32_e32 v200, v200\n\t"
      "v_mfma_f32_16x16x32_bf16 v[208:211], v[168:171], v[136:139], v[208:211]\n\t"
      "v_fma_f32 v203, v203, s36, v245\n\t"
      "v_exp_f32_e32 v203, v203\n\t"
      "v_fma_f32 v205, v205, s36, v245\n\t"
      "v_mfma_f32_16x16x32_bf16 v[208:211], v[172:175], v[140:143], v[208:211]\n\t"
      "v_fma_f32 v206, v206, s36, v245\n\t"
      "v_fma_f32 v207, v207, s36, v245\n\t"
      "v_fma_f32 v204, v204, s36, v245\n\t"
      "v_mfma_f32_16x16x32_bf16 v[212:215], v[144:147], v[128:131], v[180:183]\n\t"
      "v_exp_f32_e32 v205, v205\n\t"
      "v_exp_f32_e32 v206, v206\n\t"
      "v_mfma_f32_16x16x32_bf16 v[212:215], v[148:151], v[132:135], v[212:215]\n\t"
      "v_cvt_pk_bf16_f32 v220, v200, v201\n\t"
      "v_cvt_pk_bf16_f32 v221, v202, v203\n\t"
      "v_mfma_f32_16x16x32_bf16 v[212:215], v[152:155], v[136:139], v[212:215]\n\t"
      "v_exp_f32_e32 v204, v204\n\t"
      "s_nop 0\n\t"
      "v_cvt_pk_bf16_f32 v222, v204, v205\n\t"
      "v_mfma_f32_16x16x32_bf16 v[212:215], v[156:159], v[140:143], v[212:215]\n\t"
      "v_exp_f32_e32 v207, v207\n\t"
      "s_nop 0\n\t"
      "v_cvt_pk_bf16_f32 v223, v206, v207\n\t"
      "s_waitcnt vmcnt(4)\n\t"
      "s_add_u32 m0, s42, 49152\n\t"
      "s_nop 0\n\t"
      "buffer_load_dwordx4 v251, s[12:15], s31 offen lds\n\t"
      "s_addk_i32 m0, 896\n\t"
      "s_nop 0\n\t"
      "buffer_load_dwordx4 v251, s[12:15], s31 offen offset:128 lds\n\t"
      "s_addk_i32 m0, 7296\n\t"
      "s_nop 0\n\t"
      "buffer_load_dwordx4 v252, s[16:19], s41 offen lds\n\t"
      "s_addk_i32 m0, 896\n\t"
      "s_nop 0\n\t"
      "buffer_load_dwordx4 v252, s[16:19], s41 offen offset:128 lds\n\t"
      "s_mov_b32 s41, s31\n\t"
      "s_add_u32 s31, s31, 0x20000\n\t"
      "s_cmp_lt_u32 s42, s40\n\t"
      "s_cselect_b32 s34, s35, s58\n\t"
      "s_mov_b32 m0, s42\n\t"
      "s_add_u32 s42, s42, s34\n\t"
      "v_add_u32_e32 v248, s34, v248\n\t"
      "v_add_u32_e32 v249, s34, v249\n\t"
      "v_add_u32_e32 v250, s34, v250\n\t"
      "v_max3_f32 v246, v208, v209, v210\n\t"
      "v_max3_f32 v253, v211, v212, v213\n\t"
      "v_max3_f32 v246, v246, v214, v215\n\t"
      "v_max_f32_e32 v246, v246, v253\n\t"
      "v_mov_b32_e32 v253, v246\n\t"
      "s_nop 1\n\t"
      "v_permlane16_swap_b32_e32 v246, v253\n\t"
      "s_nop 0\n\t"
      "v_max_f32_e32 v246, v246, v253\n\t"
      "v_mov_b32_e32 v253, v246\n\t"
      "s_nop 1\n\t"
      "v_permlane32_swap_b32_e32 v246, v253\n\t"
      "s_nop 0\n\t"
      "v_max_f32_e32 v246, v246, v253\n\t"
      "v_mul_f32_e64 v246, -v246, s36\n\t"
      "v_fma_f32 v214, v214, s36, v246\n\t"
      "v_fma_f32 v213, v213, s36, v246\n\t"
      "v_exp_f32_e32 v213, v213\n\t"
      "v_exp_f32_e32 v214, v214\n\t"
      "v_fma_f32 v215, v215, s36, v246\n\t"
      "v_exp_f32_e32 v215, v215\n\t"
      "v_fma_f32 v210, v210, s36, v246\n\t"
      "v_exp_f32_e32 v210, v210\n\t"
      "v_fma_f32 v208, v208, s36, v246\n\t"
      "v_fma_f32 v209, v209, s36, v246\n\t"
      "v_exp_f32_e32 v208, v208\n\t"
      "s_cmp_lg_u32 s30, 1\n\t"
      "s_cbranch_scc1 L_pw_B3_%=_0\n\t"
      "s_waitcnt vmcnt(0)\n\t"
      "L_pw_B3_%=_0:\n\t"
      "s_waitcnt vmcnt(8)\n\t"
      "s_barrier\n\t"
      "ds_read_b128 v[160:163], v248 offset:0\n\t"
      "ds_read_b128 v[164:167], v248 offset:512\n\t"
      "ds_read_b128 v[168:171], v248 offset:1024\n\t"
      "ds_read_b128 v[172:175], v248 offset:1536\n\t"
      "ds_read_b128 v[144:147], v248 offset:4096\n\t"
      "ds_read_b128 v[148:151], v248 offset:4608\n\t"
      "ds_read_b128 v[152:155], v248 offset:5120\n\t"
      "ds_read_b128 v[156:159], v248 offset:5632\n\t"
      "s_cmp_lt_u32 s30, 4\n\t"
      "s_cbranch_scc1 L_pnd_B3_%=\n\t"
      "s_nop 0\n\t"
      "buffer_load_dwordx4 v251, s[12:15], s31 offen lds\n\t"
      "s_addk_i32 m0, 896\n\t"
      "s_nop 0\n\t"
      "buffer_load_dwordx4 v251, s[12:15], s31 offen offset:128 lds\n\t"
      "s_addk_i32 m0, 7296\n\t"
      "s_nop 0\n\t"
      "buffer_load_dwordx4 v252, s[16:19], s41 offen lds\n\t"
      "s_addk_i32 m0, 896\n\t"
      "s_nop 0\n\t"
      "buffer_load_dwordx4 v252, s[16:19], s41 offen offset:128 lds\n\t"
      "s_mov_b32 s41, s31\n\t"
      "s_add_u32 s31, s31, 0x20000\n\t"
      "L_pnd_B3_%=:\n\t"
      "s_sub_u32 s30, s30, 1\n\t"
      "s_cmp_lt_u32 s30, 4\n\t"
      "s_waitcnt lgkmcnt(4)\n\t"
      "s_cbranch_scc1 L_s1_B3_%=\n\t"
      "L_dma_B3_%=:\n\t"
      "v_mfma_f32_16x16x32_bf16 v[192:195], v[160:163], v[96:99], 0\n\t"
      "s_cmp_lt_u32 s42, s40\n\t"
      "ds_read_b64_tr_b16 v[176:177], v249 offset:0\n\t"
      "v_cvt_pk_bf16_f32 v227, v214, v215\n\t"
      "v_mfma_f32_16x16x32_bf16 v[192:195], v[164:167], v[100:103], v[192:195]\n\t"
      "s_cselect_b32 s34, s35, s58\n\t"
      "ds_read_b64_tr_b16 v[178:179], v249 offset:4096\n\t"
      "v_add_u32_e32 v248, s34, v248\n\t"
      "v_mfma_f32_16x16x32_bf16 v[192:195], v[168:171], v[104:107], v[192:195]\n\t"
      "s_mov_b32 m0, s42\n\t"
      "v_fma_f32 v212, v212, s36, v246\n\t"
      "v_exp_f32_e32 v212, v212\n\t"
      "v_mfma_f32_16x16x32_bf16 v[192:195], v[172:175], v[108:111], v[192:195]\n\t"
      "s_waitcnt lgkmcnt(2)\n\t"
      "v_cvt_pk_bf16_f32 v226, v212, v213\n\t"
      "v_fma_f32 v211, v211, s36, v246\n\t"
      "v_mfma_f32_16x16x32_bf16 v[196:199], v[144:147], v[96:99], 0\n\t"
      "s_add_u32 s42, s42, s34\n\t"
      "ds_read_b64_tr_b16 v[180:181], v250 offset:0\n\t"
      "v_exp_f32_e32 v211, v211\n\t"
      "v_mfma_f32_16x16x32_bf16 v[196:199], v[148:151], v[100:103], v[196:199]\n\t"
      "ds_read_b64_tr_b16 v[182:183], v250 offset:4096\n\t"
      "v_cvt_pk_bf16_f32 v225, v210, v211\n\t"
      "v_mfma_f32_16x16x32_bf16 v[196:199], v[152:155], v[104:107], v[196:199]\n\t"
      "ds_read_b64_tr_b16 v[184:185], v249 offset:512\n\t"
      "v_exp_f32_e32 v209, v209\n\t"
      "v_mfma_f32_16x16x32_bf16 v[196:199], v[156:159], v[108:111], v[196:199]\n\t"
      "ds_read_b64_tr_b16 v[186:187], v249 offset:4608\n\t"
      "v_fma_f32 v194, v194, s36, v244\n\t"
      "v_mfma_f32_16x16x32_bf16 v[200:203], v[160:163], v[112:115], 0\n\t"
      "ds_read_b64_tr_b16 v[188:189], v250 offset:512\n\t"
      "v_fma_f32 v192, v192, s36, v244\n\t"
      "v_mfma_f32_16x16x32_bf16 v[200:203], v[164:167], v[116:119], v[200:203]\n\t"
      "ds_read_b64_tr_b16 v[190:191], v250 offset:4608\n\t"
      "v_fma_f32 v195, v195, s36, v244\n\t"
      "v_mfma_f32_16x16x32_bf16 v[200:203], v[168:171], v[120:123], v[200:203]\n\t"
      "v_fma_f32 v193, v193, s36, v244\n\t"
      "v_exp_f32_e32 v195, v195\n\t"
      "v_mfma_f32_16x16x32_bf16 v[200:203], v[172:175], v[124:127], v[200:203]\n\t"
      "v_exp_f32_e32 v193, v193\n\t"
      "v_mfma_f32_16x16x32_bf16 v[204:207], v[144:147], v[112:115], 0\n\t"
      "v_fma_f32 v199, v199, s36, v244\n\t"
      "v_fma_f32 v198, v198, s36, v244\n\t"
      "v_mfma_f32_16x16x32_bf16 v[204:207], v[148:151], v[116:119], v[204:207]\n\t"
      "v_fma_f32 v197, v197, s36, v244\n\t"
      "v_fma_f32 v196, v196, s36, v244\n\t"
      "v_mfma_f32_16x16x32_bf16 v[204:207], v[152:155], v[120:123], v[204:207]\n\t"
      "v_exp_f32_e32 v194, v194\n\t"
      "v_mfma_f32_16x16x32_bf16 v[204:207], v[156:159], v[124:127], v[204:207]\n\t"
      "v_exp_f32_e32 v198, v198\n\t"
      "v_mfma_f32_16x16x32_bf16 v[212:215], v[144:147], v[128:131], 0\n\t"
      "ds_read_b64_tr_b16 v[144:145], v249 offset:1024\n\t"
      "v_exp_f32_e32 v192, v192\n\t"
      "v_mfma_f32_16x16x32_bf16 v[212:215], v[148:151], v[132:135], v[212:215]\n\t"
      "ds_read_b64_tr_b16 v[146:147], v249 offset:5120\n\t"
      "v_exp_f32_e32 v199, v199\n\t"
      "v_mfma_f32_16x16x32_bf16 v[212:215], v[152:155], v[136:139], v[212:215]\n\t"
      "ds_read_b64_tr_b16 v[148:149], v250 offset:1024\n\t"
      "v_exp_f32_e32 v197, v197\n\t"
      "v_mfma_f32_16x16x32_bf16 v[212:215], v[156:159], v[140:143], v[212:215]\n\t"
      "ds_read_b64_tr_b16 v[150:151], v250 offset:5120\n\t"
      "v_cvt_pk_bf16_f32 v224, v208, v209\n\t"
      "v_fma_f32 v202, v202, s36, v245\n\t"
      "v_mfma_f32_16x16x32_bf16 v[208:211], v[160:163], v[128:131], 0\n\t"
      "ds_read_b64_tr_b16 v[152:153], v249 offset:1536\n\t"
      "v_fma_f32 v200, v200, s36, v245\n\t"
      "v_mfma_f32_16x16x32_bf16 v[208:211], v[164:167], v[132:135], v[208:211]\n\t"
      "ds_read_b64_tr_b16 v[154:155], v249 offset:5632\n\t"
      "v_fma_f32 v201, v201, s36, v245\n\t"
      "v_mfma_f32_16x16x32_bf16 v[208:211], v[168:171], v[136:139], v[208:211]\n\t"
      "ds_read_b64_tr_b16 v[156:157], v250 offset:1536\n\t"
      "v_exp_f32_e32 v201, v201\n\t"
      "v_mfma_f32_16x16x32_bf16 v[208:211], v[172:175], v[140:143], v[208:211]\n\t"
      "ds_read_b64_tr_b16 v[158:159], v250 offset:5632\n\t"
      "v_add_u32_e32 v249, s34, v249\n\t"
      "v_mfma_f32_16x16x32_bf16 v[228:231], v[240:243], v[216:219], v[228:231]\n\t"
      "s_waitcnt lgkmcnt(8)\n\t"
      "v_add_u32_e32 v250, s34, v250\n\t"
      "v_exp_f32_e32 v202, v202\n\t"
      "v_mfma_f32_16x16x32_bf16 v[0:3], v[176:179], v[216:219], v[0:3]\n\t"
      "v_exp_f32_e32 v200, v200\n\t"
      "v_mfma_f32_16x16x32_bf16 v[4:7], v[180:183], v[216:219], v[4:7]\n\t"
      "v_fma_f32 v203, v203, s36, v245\n\t"
      "v_exp_f32_e32 v203, v203\n\t"
      "v_mfma_f32_16x16x32_bf16 v[8:11], v[184:187], v[216:219], v[8:11]\n\t"
      "v_fma_f32 v205, v205, s36, v245\n\t"
      "v_fma_f32 v206, v206, s36, v245\n\t"
      "v_mfma_f32_16x16x32_bf16 v[12:15], v[188:191], v[216:219], v[12:15]\n\t"
      "s_waitcnt lgkmcnt(4)\n\t"
      "v_fma_f32 v207, v207, s36, v245\n\t"
      "v_fma_f32 v204, v204, s36, v245\n\t"
      "v_mfma_f32_16x16x32_bf16 v[16:19], v[144:147], v[216:219], v[16:19]\n\t"
      "v_exp_f32_e32 v205, v205\n\t"
      "v_mfma_f32_16x16x32_bf16 v[20:23], v[148:151], v[216:219], v[20:23]\n\t"
      "s_waitcnt vmcnt(8) lgkmcnt(0)\n\t"
      "s_barrier\n\t"
      "v_mfma_f32_16x16x32_bf16 v[24:27], v[152:155], v[216:219], v[24:27]\n\t"
      "s_sub_u32 s30, s30, 1\n\t"
      "buffer_load_dwordx4 v251, s[12:15], s31 offen lds\n\t"
      "v_exp_f32_e32 v206, v206\n\t"
      "v_mfma_f32_16x16x32_bf16 v[28:31], v[156:159], v[216:219], v[28:31]\n\t"
      "s_addk_i32 m0, 896\n\t"
      "ds_read_b128 v[160:163], v248 offset:0\n\t"
      "v_cvt_pk_bf16_f32 v216, v192, v193\n\t"
      "v_mfma_f32_16x16x32_bf16 v[232:235], v[240:243], v[220:223], v[232:235]\n\t"
      "buffer_load_dwordx4 v251, s[12:15], s31 offen offset:128 lds\n\t"
      "v_cvt_pk_bf16_f32 v217, v194, v195\n\t"
      "v_mfma_f32_16x16x32_bf16 v[32:35], v[176:179], v[220:223], v[32:35]\n\t"
      "s_addk_i32 m0, 7296\n\t"
      "ds_read_b128 v[164:167], v248 offset:512\n\t"
      "v_cvt_pk_bf16_f32 v219, v198, v199\n\t"
      "v_mfma_f32_16x16x32_bf16 v[36:39], v[180:183], v[220:223], v[36:39]\n\t"
      "buffer_load_dwordx4 v252, s[16:19], s41 offen lds\n\t"
      "v_exp_f32_e32 v196, v196\n\t"
      "v_mfma_f32_16x16x32_bf16 v[40:43], v[184:187], v[220:223], v[40:43]\n\t"
      "s_addk_i32 m0, 896\n\t"
      "ds_read_b128 v[168:171], v248 offset:1024\n\t"
      "v_cvt_pk_bf16_f32 v218, v196, v197\n\t"
      "v_mfma_f32_16x16x32_bf16 v[44:47], v[188:191], v[220:223], v[44:47]\n\t"
      "ds_read_b128 v[172:175], v248 offset:1536\n\t"
      "v_fma_f32 v214, v214, s36, v246\n\t"
      "v_mfma_f32_16x16x32_bf16 v[48:51], v[144:147], v[220:223], v[48:51]\n\t"
      "buffer_load_dwordx4 v252, s[16:19], s41 offen offset:128 lds\n\t"
      "v_fma_f32 v213, v213, s36, v246\n\t"
      "v_mfma_f32_16x16x32_bf16 v[52:55], v[148:151], v[220:223], v[52:55]\n\t"
      "s_mov_b32 s41, s31\n\t"
      "v_exp_f32_e32 v213, v213\n\t"
      "v_mfma_f32_16x16x32_bf16 v[56:59], v[152:155], v[220:223], v[56:59]\n\t"
      "s_add_u32 s31, s31, 0x20000\n\t"
      "v_exp_f32_e32 v214, v214\n\t"
      "v_mfma_f32_16x16x32_bf16 v[60:63], v[156:159], v[220:223], v[60:63]\n\t"
      "v_cvt_pk_bf16_f32 v220, v200, v201\n\t"
      "v_cvt_pk_bf16_f32 v221, v202, v203\n\t"
      "v_mfma_f32_16x16x32_bf16 v[80:83], v[144:147], v[224:227], v[80:83]\n\t"
      "ds_read_b128 v[144:147], v248 offset:4096\n\t"
      "v_exp_f32_e32 v204, v204\n\t"
      "v_mfma_f32_16x16x32_bf16 v[84:87], v[148:151], v[224:227], v[84:87]\n\t"
      "ds_read_b128 v[148:151], v248 offset:4608\n\t"
      "v_cvt_pk_bf16_f32 v222, v204, v205\n\t"
      "v_mfma_f32_16x16x32_bf16 v[88:91], v[152:155], v[224:227], v[88:91]\n\t"
      "ds_read_b128 v[152:155], v248 offset:5120\n\t"
      "v_fma_f32 v215, v215, s36, v246\n\t"
      "v_mfma_f32_16x16x32_bf16 v[92:95], v[156:159], v[224:227], v[92:95]\n\t"
      "ds_read_b128 v[156:159], v248 offset:5632\n\t"
      "v_exp_f32_e32 v215, v215\n\t"
      "v_mfma_f32_16x16x32_bf16 v[64:67], v[176:179], v[224:227], v[64:67]\n\t"
      "v_fma_f32 v210, v210, s36, v246\n\t"
      "v_exp_f32_e32 v207, v207\n\t"
      "v_mfma_f32_16x16x32_bf16 v[68:71], v[180:183], v[224:227], v[68:71]\n\t"
      "v_cvt_pk_bf16_f32 v223, v206, v207\n\t"
      "v_exp_f32_e32 v210, v210\n\t"
      "v_mfma_f32_16x16x32_bf16 v[72:75], v[184:187], v[224:227], v[72:75]\n\t"
      "v_fma_f32 v208, v208, s36, v246\n\t"
      "v_fma_f32 v209, v209, s36, v246\n\t"
      "v_mfma_f32_16x16x32_bf16 v[76:79], v[188:191], v[224:227], v[76:79]\n\t"
      "s_cmp_ge_u32 s30, s59\n\t"
      "v_exp_f32_e32 v208, v208\n\t"
      "v_mfma_f32_16x16x32_bf16 v[236:239], v[240:243], v[224:227], v[236:239]\n\t"
      "s_waitcnt lgkmcnt(4)\n\t"
      "s_cbranch_scc1 L_dma_B3_%=\n\t"
      "L_s1_B3_%=:\n\t"
      "v_mbcnt_lo_u32_b32 v252, -1, 0\n\t"
      "v_mbcnt_hi_u32_b32 v252, -1, v252\n\t"
      "v_and_b32_e32 v253, 7, v252\n\t"
      "v_bfe_u32 v251, v252, 3, 1\n\t"
      "v_lshl_or_b32 v253, v253, 1, v251\n\t"
      "v_lshrrev_b32_e32 v251, 4, v252\n\t"
      "v_xor_b32_e32 v253, v253, v251\n\t"
      "v_and_b32_e32 v251, 15, v252\n\t"
      "v_lshlrev_b32_e32 v251, 4, v251\n\t"
      "v_add_lshl_u32 v253, v253, v251, 3\n\t"
      "v_lshrrev_b32_e32 v251, 3, v252\n\t"
      "s_movk_i32 s37, 0xf8\n\t"
      "v_mad_u32_u24 v254, v251, s37, v252\n\t"
      "v_xor_b32_e32 v251, v251, v252\n\t"
      "v_lshlrev_b32_e32 v251, 4, v251\n\t"
      "v_lshlrev_b32_e32 v252, 4, v254\n\t"
      "s_cmp_gt_u32 s94, 8\n\t"
      "s_cbranch_scc1 L_kp_B3_%=\n\t"
      "s_setprio 0\n\t"
      "L_kp_B3_%=:\n\t"
      "s_cmp_eq_u32 s30, 0\n\t"
      "s_cbranch_scc1 L_dr_B3_%=\n\t"
      "L_nod_B3_%=:\n\t"
      "v_mfma_f32_16x16x32_bf16 v[192:195], v[160:163], v[96:99], 0\n\t"
      "s_cmp_lt_u32 s42, s40\n\t"
      "ds_read_b64_tr_b16 v[176:177], v249 offset:0\n\t"
      "v_cvt_pk_bf16_f32 v227, v214, v215\n\t"
      "v_mfma_f32_16x16x32_bf16 v[192:195], v[164:167], v[100:103], v[192:195]\n\t"
      "s_cselect_b32 s34, s35, s58\n\t"
      "ds_read_b64_tr_b16 v[178:179], v249 offset:4096\n\t"
      "v_add_u32_e32 v248, s34, v248\n\t"
      "v_mfma_f32_16x16x32_bf16 v[192:195], v[168:171], v[104:107], v[192:195]\n\t"
      "s_mov_b32 m0, s42\n\t"
      "v_fma_f32 v212, v212, s36, v246\n\t"
      "v_exp_f32_e32 v212, v212\n\t"
      "v_mfma_f32_16x16x32_bf16 v[192:195], v[172:175], v[108:111], v[192:195]\n\t"
      "s_waitcnt lgkmcnt(2)\n\t"
      "v_cvt_pk_bf16_f32 v226, v212, v213\n\t"
      "v_fma_f32 v211, v211, s36, v246\n\t"
      "v_mfma_f32_16x16x32_bf16 v[196:199], v[144:147], v[96:99], 0\n\t"
      "s_add_u32 s42, s42, s34\n\t"
      "ds_read_b64_tr_b16 v[180:181], v250 offset:0\n\t"
      "v_exp_f32_e32 v211, v211\n\t"
      "v_mfma_f32_16x16x32_bf16 v[196:199], v[148:151], v[100:103], v[196:199]\n\t"
      "ds_read_b64_tr_b16 v[182:183], v250 offset:4096\n\t"
      "v_cvt_pk_bf16_f32 v225, v210, v211\n\t"
      "v_mfma_f32_16x16x32_bf16 v[196:199], v[152:155], v[104:107], v[196:199]\n\t"
      "ds_read_b64_tr_b16 v[184:185], v249 offset:512\n\t"
      "v_exp_f32_e32 v209, v209\n\t"
      "v_mfma_f32_16x16x32_bf16 v[196:199], v[156:159], v[108:111], v[196:199]\n\t"
      "ds_read_b64_tr_b16 v[186:187], v249 offset:4608\n\t"
      "v_fma_f32 v194, v194, s36, v244\n\t"
      "v_mfma_f32_16x16x32_bf16 v[200:203], v[160:163], v[112:115], 0\n\t"
      "ds_read_b64_tr_b16 v[188:189], v250 offset:512\n\t"
      "v_fma_f32 v192, v192, s36, v244\n\t"
      "v_mfma_f32_16x16x32_bf16 v[200:203], v[164:167], v[116:119], v[200:203]\n\t"
      "ds_read_b64_tr_b16 v[190:191], v250 offset:4608\n\t"
      "v_fma_f32 v195, v195, s36, v244\n\t"
      "v_mfma_f32_16x16x32_bf16 v[200:203], v[168:171], v[120:123], v[200:203]\n\t"
      "v_fma_f32 v193, v193, s36, v244\n\t"
      "v_exp_f32_e32 v195, v195\n\t"
      "v_mfma_f32_16x16x32_bf16 v[200:203], v[172:175], v[124:127], v[200:203]\n\t"
      "v_exp_f32_e32 v193, v193\n\t"
      "v_mfma_f32_16x16x32_bf16 v[204:207], v[144:147], v[112:115], 0\n\t"
      "v_fma_f32 v199, v199, s36, v244\n\t"
      "v_fma_f32 v198, v198, s36, v244\n\t"
      "v_mfma_f32_16x16x32_bf16 v[204:207], v[148:151], v[116:119], v[204:207]\n\t"
      "v_fma_f32 v197, v197, s36, v244\n\t"
      "v_fma_f32 v196, v196, s36, v244\n\t"
      "v_mfma_f32_16x16x32_bf16 v[204:207], v[152:155], v[120:123], v[204:207]\n\t"
      "v_exp_f32_e32 v194, v194\n\t"
      "v_mfma_f32_16x16x32_bf16 v[204:207], v[156:159], v[124:127], v[204:207]\n\t"
      "v_exp_f32_e32 v198, v198\n\t"
      "v_mfma_f32_16x16x32_bf16 v[212:215], v[144:147], v[128:131], 0\n\t"
      "ds_read_b64_tr_b16 v[144:145], v249 offset:1024\n\t"
      "v_exp_f32_e32 v192, v192\n\t"
      "v_mfma_f32_16x16x32_bf16 v[212:215], v[148:151], v[132:135], v[212:215]\n\t"
      "ds_read_b64_tr_b16 v[146:147], v249 offset:5120\n\t"
      "v_exp_f32_e32 v199, v199\n\t"
      "v_mfma_f32_16x16x32_bf16 v[212:215], v[152:155], v[136:139], v[212:215]\n\t"
      "ds_read_b64_tr_b16 v[148:149], v250 offset:1024\n\t"
      "v_exp_f32_e32 v197, v197\n\t"
      "v_mfma_f32_16x16x32_bf16 v[212:215], v[156:159], v[140:143], v[212:215]\n\t"
      "ds_read_b64_tr_b16 v[150:151], v250 offset:5120\n\t"
      "v_cvt_pk_bf16_f32 v224, v208, v209\n\t"
      "v_fma_f32 v202, v202, s36, v245\n\t"
      "v_mfma_f32_16x16x32_bf16 v[208:211], v[160:163], v[128:131], 0\n\t"
      "ds_read_b64_tr_b16 v[152:153], v249 offset:1536\n\t"
      "v_fma_f32 v200, v200, s36, v245\n\t"
      "v_mfma_f32_16x16x32_bf16 v[208:211], v[164:167], v[132:135], v[208:211]\n\t"
      "ds_read_b64_tr_b16 v[154:155], v249 offset:5632\n\t"
      "v_fma_f32 v201, v201, s36, v245\n\t"
      "v_mfma_f32_16x16x32_bf16 v[208:211], v[168:171], v[136:139], v[208:211]\n\t"
      "ds_read_b64_tr_b16 v[156:157], v250 offset:1536\n\t"
      "v_exp_f32_e32 v201, v201\n\t"
      "v_mfma_f32_16x16x32_bf16 v[208:211], v[172:175], v[140:143], v[208:211]\n\t"
      "ds_read_b64_tr_b16 v[158:159], v250 offset:5632\n\t"
      "v_add_u32_e32 v249, s34, v249\n\t"
      "v_mfma_f32_16x16x32_bf16 v[228:231], v[240:243], v[216:219], v[228:231]\n\t"
      "s_waitcnt lgkmcnt(8)\n\t"
      "v_add_u32_e32 v250, s34, v250\n\t"
      "v_exp_f32_e32 v202, v202\n\t"
      "v_mfma_f32_16x16x32_bf16 v[0:3], v[176:179], v[216:219], v[0:3]\n\t"
      "v_exp_f32_e32 v200, v200\n\t"
      "v_mfma_f32_16x16x32_bf16 v[4:7], v[180:183], v[216:219], v[4:7]\n\t"
      "v_fma_f32 v203, v203, s36, v245\n\t"
      "v_exp_f32_e32 v203, v203\n\t"
      "v_mfma_f32_16x16x32_bf16 v[8:11], v[184:187], v[216:219], v[8:11]\n\t"
      "v_fma_f32 v205, v205, s36, v245\n\t"
      "v_fma_f32 v206, v206, s36, v245\n\t"
      "v_mfma_f32_16x16x32_bf16 v[12:15], v[188:191], v[216:219], v[12:15]\n\t"
      "s_waitcnt lgkmcnt(4)\n\t"
      "v_fma_f32 v207, v207, s36, v245\n\t"
      "v_fma_f32 v204, v204, s36, v245\n\t"
      "v_mfma_f32_16x16x32_bf16 v[16:19], v[144:147], v[216:219], v[16:19]\n\t"
      "v_exp_f32_e32 v205, v205\n\t"
      "v_mfma_f32_16x16x32_bf16 v[20:23], v[148:151], v[216:219], v[20:23]\n\t"
      "s_waitcnt vmcnt(0) lgkmcnt(0)\n\t"
      "s_barrier\n\t"
      "v_mfma_f32_16x16x32_bf16 v[24:27], v[152:155], v[216:219], v[24:27]\n\t"
      "s_sub_u32 s30, s30, 1\n\t"
      "v_exp_f32_e32 v206, v206\n\t"
      "v_mfma_f32_16x16x32_bf16 v[28:31], v[156:159], v[216:219], v[28:31]\n\t"
      "ds_read_b128 v[160:163], v248 offset:0\n\t"
      "v_cvt_pk_bf16_f32 v216, v192, v193\n\t"
      "v_mfma_f32_16x16x32_bf16 v[232:235], v[240:243], v[220:223], v[232:235]\n\t"
      "v_cvt_pk_bf16_f32 v217, v194, v195\n\t"
      "v_mfma_f32_16x16x32_bf16 v[32:35], v[176:179], v[220:223], v[32:35]\n\t"
      "ds_read_b128 v[164:167], v248 offset:512\n\t"
      "v_cvt_pk_bf16_f32 v219, v198, v199\n\t"
      "v_mfma_f32_16x16x32_bf16 v[36:39], v[180:183], v[220:223], v[36:39]\n\t"
      "v_exp_f32_e32 v196, v196\n\t"
      "v_mfma_f32_16x16x32_bf16 v[40:43], v[184:187], v[220:223], v[40:43]\n\t"
      "ds_read_b128 v[168:171], v248 offset:1024\n\t"
      "v_cvt_pk_bf16_f32 v218, v196, v197\n\t"
      "v_mfma_f32_16x16x32_bf16 v[44:47], v[188:191], v[220:223], v[44:47]\n\t"
      "ds_read_b128 v[172:175], v248 offset:1536\n\t"
      "v_fma_f32 v214, v214, s36, v246\n\t"
      "v_mfma_f32_16x16x32_bf16 v[48:51], v[144:147], v[220:223], v[48:51]\n\t"
      "v_fma_f32 v213, v213, s36, v246\n\t"
      "v_mfma_f32_16x16x32_bf16 v[52:55], v[148:151], v[220:223], v[52:55]\n\t"
      "v_exp_f32_e32 v213, v213\n\t"
      "v_mfma_f32_16x16x32_bf16 v[56:59], v[152:155], v[220:223], v[56:59]\n\t"
      "v_exp_f32_e32 v214, v214\n\t"
      "v_mfma_f32_16x16x32_bf16 v[60:63], v[156:159], v[220:223], v[60:63]\n\t"
      "v_cvt_pk_bf16_f32 v220, v200, v201\n\t"
      "v_cvt_pk_bf16_f32 v221, v202, v203\n\t"
      "v_mfma_f32_16x16x32_bf16 v[80:83], v[144:147], v[224:227], v[80:83]\n\t"
      "ds_read_b128 v[144:147], v248 offset:4096\n\t"
      "v_exp_f32_e32 v204, v204\n\t"
      "v_mfma_f32_16x16x32_bf16 v[84:87], v[148:151], v[224:227], v[84:87]\n\t"
      "ds_read_b128 v[148:151], v248 offset:4608\n\t"
      "v_cvt_pk_bf16_f32 v222, v204, v205\n\t"
      "v_mfma_f32_16x16x32_bf16 v[88:91], v[152:155], v[224:227], v[88:91]\n\t"
      "ds_read_b128 v[152:155], v248 offset:5120\n\t"
      "v_fma_f32 v215, v215, s36, v246\n\t"
      "v_mfma_f32_16x16x32_bf16 v[92:95], v[156:159], v[224:227], v[92:95]\n\t"
      "ds_read_b128 v[156:159], v248 offset:5632\n\t"
      "v_exp_f32_e32 v215, v215\n\t"
      "v_mfma_f32_16x16x32_bf16 v[64:67], v[176:179], v[224:227], v[64:67]\n\t"
      "v_fma_f32 v210, v210, s36, v246\n\t"
      "v_exp_f32_e32 v207, v207\n\t"
      "v_mfma_f32_16x16x32_bf16 v[68:71], v[180:183], v[224:227], v[68:71]\n\t"
      "v_cvt_pk_bf16_f32 v223, v206, v207\n\t"
      "v_exp_f32_e32 v210, v210\n\t"
      "v_mfma_f32_16x16x32_bf16 v[72:75], v[184:187], v[224:227], v[72:75]\n\t"
      "v_fma_f32 v208, v208, s36, v246\n\t"
      "v_fma_f32 v209, v209, s36, v246\n\t"
      "v_mfma_f32_16x16x32_bf16 v[76:79], v[188:191], v[224:227], v[76:79]\n\t"
      "s_cmp_ge_u32 s30, 1\n\t"
      "v_exp_f32_e32 v208, v208\n\t"
      "v_mfma_f32_16x16x32_bf16 v[236:239], v[240:243], v[224:227], v[236:239]\n\t"
      "s_waitcnt lgkmcnt(4)\n\t"
      "s_cbranch_scc1 L_nod_B3_%=\n\t"
      "L_dr_B3_%=:\n\t"
      "v_mfma_f32_16x16x32_bf16 v[228:231], v[240:243], v[216:219], v[228:231]\n\t"
      "ds_read_b64_tr_b16 v[176:177], v249 offset:0\n\t"
      "ds_read_b64_tr_b16 v[178:179], v249 offset:4096\n\t"
      "v_mfma_f32_16x16x32_bf16 v[232:235], v[240:243], v[220:223], v[232:235]\n\t"
      "ds_read_b64_tr_b16 v[180:181], v250 offset:0\n\t"
      "ds_read_b64_tr_b16 v[182:183], v250 offset:4096\n\t"
      "ds_read_b64_tr_b16 v[184:185], v249 offset:512\n\t"
      "ds_read_b64_tr_b16 v[186:187], v249 offset:4608\n\t"
      "ds_read_b64_tr_b16 v[188:189], v250 offset:512\n\t"
      "ds_read_b64_tr_b16 v[190:191], v250 offset:4608\n\t"
      "ds_read_b64_tr_b16 v[144:145], v249 offset:1024\n\t"
      "ds_read_b64_tr_b16 v[146:147], v249 offset:5120\n\t"
      "ds_read_b64_tr_b16 v[148:149], v250 offset:1024\n\t"
      "v_fma_f32 v212, v212, s36, v246\n\t"
      "v_fma_f32 v211, v211, s36, v246\n\t"
      "v_exp_f32_e32 v212, v212\n\t"
      "v_exp_f32_e32 v211, v211\n\t"
      "v_exp_f32_e32 v209, v209\n\t"
      "v_cvt_pk_bf16_f32 v227, v214, v215\n\t"
      "v_cvt_pk_bf16_f32 v226, v212, v213\n\t"
      "v_cvt_pk_bf16_f32 v225, v210, v211\n\t"
      "v_cvt_pk_bf16_f32 v224, v208, v209\n\t"
      "ds_read_b64_tr_b16 v[150:151], v250 offset:5120\n\t"
      "s_nop 0\n\t"
      "v_mfma_f32_16x16x32_bf16 v[236:239], v[240:243], v[224:227], v[236:239]\n\t"
      "ds_read_b64_tr_b16 v[152:153], v249 offset:1536\n\t"
      "ds_read_b64_tr_b16 v[154:155], v249 offset:5632\n\t"
      "s_waitcnt lgkmcnt(12)\n\t"
      "v_mfma_f32_16x16x32_bf16 v[0:3], v[176:179], v[216:219], v[0:3]\n\t"
      "ds_read_b64_tr_b16 v[156:157], v250 offset:1536\n\t"
      "ds_read_b64_tr_b16 v[158:159], v250 offset:5632\n\t"
      "s_waitcnt lgkmcnt(12)\n\t"
      "v_mfma_f32_16x16x32_bf16 v[4:7], v[180:183], v[216:219], v[4:7]\n\t"
      "s_lshr_b32 s37, s42, 14\n\t"
      "v_rcp_f32_e32 v230, v228\n\t"
      "s_waitcnt lgkmcnt(8)\n\t"
      "v_mfma_f32_16x16x32_bf16 v[8:11], v[184:187], v[216:219], v[8:11]\n\t"
      "s_add_u32 s37, s37, s26\n\t"
      "s_add_u32 s37, s37, 1\n\t"
      "s_sub_u32 s38, s37, 5\n\t"
      "s_cselect_b32 s37, s37, s38\n\t"
      "v_mfma_f32_16x16x32_bf16 v[12:15], v[188:191], v[216:219], v[12:15]\n\t"
      "s_lshl_b32 s42, s37, 14\n\t"
      "v_mul_f32_e32 v0, v230, v0\n\t"
      "v_mul_f32_e32 v1, v230, v1\n\t"
      "s_waitcnt lgkmcnt(4)\n\t"
      "v_mfma_f32_16x16x32_bf16 v[16:19], v[144:147], v[216:219], v[16:19]\n\t"
      "v_mul_f32_e32 v2, v230, v2\n\t"
      "v_mul_f32_e32 v3, v230, v3\n\t"
      "v_mfma_f32_16x16x32_bf16 v[32:35], v[176:179], v[220:223], v[32:35]\n\t"
      "v_mul_f32_e32 v4, v230, v4\n\t"
      "v_mul_f32_e32 v5, v230, v5\n\t"
      "v_mfma_f32_16x16x32_bf16 v[20:23], v[148:151], v[216:219], v[20:23]\n\t"
      "v_cvt_pk_bf16_f32 v0, v0, v1\n\t"
      "v_cvt_pk_bf16_f32 v1, v2, v3\n\t"
      "s_waitcnt lgkmcnt(0)\n\t"
      "v_mfma_f32_16x16x32_bf16 v[24:27], v[152:155], v[216:219], v[24:27]\n\t"
      "v_mul_f32_e32 v6, v230, v6\n\t"
      "v_mul_f32_e32 v7, v230, v7\n\t"
      "v_mfma_f32_16x16x32_bf16 v[92:95], v[156:159], v[224:227], v[92:95]\n\t"
      "v_mul_f32_e32 v8, v230, v8\n\t"
      "v_mul_f32_e32 v9, v230, v9\n\t"
      "v_mfma_f32_16x16x32_bf16 v[28:31], v[156:159], v[216:219], v[28:31]\n\t"
      "v_cvt_pk_bf16_f32 v4, v4, v5\n\t"
      "v_cvt_pk_bf16_f32 v5, v6, v7\n\t"
      "v_mfma_f32_16x16x32_bf16 v[36:39], v[180:183], v[220:223], v[36:39]\n\t"
      "v_mul_f32_e32 v10, v230, v10\n\t"
      "v_mul_f32_e32 v11, v230, v11\n\t"
      "v_mfma_f32_16x16x32_bf16 v[40:43], v[184:187], v[220:223], v[40:43]\n\t"
      "v_mul_f32_e32 v12, v230, v12\n\t"
      "v_mul_f32_e32 v13, v230, v13\n\t"
      "v_mfma_f32_16x16x32_bf16 v[44:47], v[188:191], v[220:223], v[44:47]\n\t"
      "v_cvt_pk_bf16_f32 v8, v8, v9\n\t"
      "v_cvt_pk_bf16_f32 v9, v10, v11\n\t"
      "v_mfma_f32_16x16x32_bf16 v[48:51], v[144:147], v[220:223], v[48:51]\n\t"
      "v_mul_f32_e32 v14, v230, v14\n\t"
      "v_mul_f32_e32 v15, v230, v15\n\t"
      "v_mfma_f32_16x16x32_bf16 v[52:55], v[148:151], v[220:223], v[52:55]\n\t"
      "v_cvt_pk_bf16_f32 v12, v12, v13\n\t"
      "v_cvt_pk_bf16_f32 v13, v14, v15\n\t"
      "v_mfma_f32_16x16x32_bf16 v[56:59], v[152:155], v[220:223], v[56:59]\n\t"
      "v_add_u32_e32 v253, s42, v253\n\t"
      "v_add_u32_e32 v251, s42, v251\n\t"
      "v_mfma_f32_16x16x32_bf16 v[60:63], v[156:159], v[220:223], v[60:63]\n\t"
      "v_xor_b32_e32 v248, 32, v253\n\t"
      "v_xor_b32_e32 v254, 64, v253\n\t"
      "v_mfma_f32_16x16x32_bf16 v[64:67], v[176:179], v[224:227], v[64:67]\n\t"
      "v_xor_b32_e32 v143, 96, v253\n\t"
      "ds_write_b64 v253, v[0:1] offset:0\n\t"
      "ds_write_b64 v248, v[4:5] offset:0\n\t"
      "ds_write_b64 v254, v[8:9] offset:0\n\t"
      "ds_write_b64 v143, v[12:13] offset:0\n\t"
      "v_mfma_f32_16x16x32_bf16 v[68:71], v[180:183], v[224:227], v[68:71]\n\t"
      "ds_read_b128 v[96:99], v251 offset:0\n\t"
      "ds_read_b128 v[100:103], v251 offset:1024\n\t"
      "v_mfma_f32_16x16x32_bf16 v[72:75], v[184:187], v[224:227], v[72:75]\n\t"
      "v_rcp_f32_e32 v234, v232\n\t"
      "v_mfma_f32_16x16x32_bf16 v[76:79], v[188:191], v[224:227], v[76:79]\n\t"
      "v_rcp_f32_e32 v238, v236\n\t"
      "v_mfma_f32_16x16x32_bf16 v[80:83], v[144:147], v[224:227], v[80:83]\n\t"
      "v_add_f32_e32 v229, v228, v232\n\t"
      "v_add_f32_e32 v229, v229, v236\n\t"
      "v_add_f32_e32 v229, v229, v230\n\t"
      "v_add_f32_e32 v229, v229, v234\n\t"
      "v_add_f32_e32 v229, v229, v238\n\t"
      "v_cmp_nge_f32_e32 vcc, 0x4f800000, v229\n\t"
      "v_mfma_f32_16x16x32_bf16 v[84:87], v[148:151], v[224:227], v[84:87]\n\t"
      "s_mov_b32 s39, 1\n\t"
      "s_cbranch_vccnz L_end_%=\n\t"
      "s_mov_b32 s39, 0\n\t"
      "v_mfma_f32_16x16x32_bf16 v[88:91], v[152:155], v[224:227], v[88:91]\n\t"
      "s_waitcnt lgkmcnt(1)\n\t"
      "buffer_store_dwordx4 v[96:99], v252, s[20:23], s27 offen nt sc1\n\t"
      "s_add_u32 s38, s27, 0x8000\n\t"
      "s_waitcnt lgkmcnt(0)\n\t"
      "v_mov_b64_e32 v[104:105], v[100:101]\n\t"
      "buffer_store_dwordx4 v[102:105], v252, s[20:23], s38 offen nt sc1\n\t"
      "v_pk_mul_f32 v[16:17], v[16:17], v[230:231] op_sel_hi:[1,0]\n\t"
      "v_pk_mul_f32 v[18:19], v[18:19], v[230:231] op_sel_hi:[1,0]\n\t"
      "v_cvt_pk_bf16_f32 v16, v16, v17\n\t"
      "v_cvt_pk_bf16_f32 v17, v18, v19\n\t"
      "v_pk_mul_f32 v[20:21], v[20:21], v[230:231] op_sel_hi:[1,0]\n\t"
      "v_pk_mul_f32 v[22:23], v[22:23], v[230:231] op_sel_hi:[1,0]\n\t"
      "v_cvt_pk_bf16_f32 v20, v20, v21\n\t"
      "v_cvt_pk_bf16_f32 v21, v22, v23\n\t"
      "v_pk_mul_f32 v[24:25], v[24:25], v[230:231] op_sel_hi:[1,0]\n\t"
      "v_pk_mul_f32 v[26:27], v[26:27], v[230:231] op_sel_hi:[1,0]\n\t"
      "v_cvt_pk_bf16_f32 v24, v24, v25\n\t"
      "v_cvt_pk_bf16_f32 v25, v26, v27\n\t"
      "v_pk_mul_f32 v[28:29], v[28:29], v[230:231] op_sel_hi:[1,0]\n\t"
      "v_pk_mul_f32 v[30:31], v[30:31], v[230:231] op_sel_hi:[1,0]\n\t"
      "v_cvt_pk_bf16_f32 v28, v28, v29\n\t"
      "v_cvt_pk_bf16_f32 v29, v30, v31\n\t"
      "ds_write_b64 v253, v[16:17] offset:2048\n\t"
      "ds_write_b64 v248, v[20:21] offset:2048\n\t"
      "ds_write_b64 v254, v[24:25] offset:2048\n\t"
      "ds_write_b64 v143, v[28:29] offset:2048\n\t"
      "ds_read_b128 v[106:109], v251 offset:2048\n\t"
      "ds_read_b128 v[110:113], v251 offset:3072\n\t"
      "v_pk_mul_f32 v[32:33], v[32:33], v[234:235] op_sel_hi:[1,0]\n\t"
      "v_pk_mul_f32 v[34:35], v[34:35], v[234:235] op_sel_hi:[1,0]\n\t"
      "v_cvt_pk_bf16_f32 v32, v32, v33\n\t"
      "v_cvt_pk_bf16_f32 v33, v34, v35\n\t"
      "v_pk_mul_f32 v[36:37], v[36:37], v[234:235] op_sel_hi:[1,0]\n\t"
      "v_pk_mul_f32 v[38:39], v[38:39], v[234:235] op_sel_hi:[1,0]\n\t"
      "v_cvt_pk_bf16_f32 v36, v36, v37\n\t"
      "v_cvt_pk_bf16_f32 v37, v38, v39\n\t"
      "v_pk_mul_f32 v[40:41], v[40:41], v[234:235] op_sel_hi:[1,0]\n\t"
      "v_pk_mul_f32 v[42:43], v[42:43], v[234:235] op_sel_hi:[1,0]\n\t"
      "v_cvt_pk_bf16_f32 v40, v40, v41\n\t"
      "v_cvt_pk_bf16_f32 v41, v42, v43\n\t"
      "v_pk_mul_f32 v[44:45], v[44:45], v[234:235] op_sel_hi:[1,0]\n\t"
      "v_pk_mul_f32 v[46:47], v[46:47], v[234:235] op_sel_hi:[1,0]\n\t"
      "v_cvt_pk_bf16_f32 v44, v44, v45\n\t"
      "v_cvt_pk_bf16_f32 v45, v46, v47\n\t"
      "ds_write_b64 v253, v[32:33] offset:4096\n\t"
      "ds_write_b64 v248, v[36:37] offset:4096\n\t"
      "ds_write_b64 v254, v[40:41] offset:4096\n\t"
      "ds_write_b64 v143, v[44:45] offset:4096\n\t"
      "ds_read_b128 v[116:119], v251 offset:4096\n\t"
      "ds_read_b128 v[120:123], v251 offset:5120\n\t"
      "s_waitcnt lgkmcnt(7)\n\t"
      "buffer_store_dwordx4 v[106:109], v252, s[20:23], s27 offen offset:128 nt sc1\n\t"
      "s_waitcnt lgkmcnt(6)\n\t"
      "v_mov_b64_e32 v[114:115], v[110:111]\n\t"
      "buffer_store_dwordx4 v[112:115], v252, s[20:23], s38 offen offset:128 nt sc1\n\t"
      "s_add_u32 s27, s27, 0x10000\n\t"
      "v_pk_mul_f32 v[48:49], v[48:49], v[234:235] op_sel_hi:[1,0]\n\t"
      "v_pk_mul_f32 v[50:51], v[50:51], v[234:235] op_sel_hi:[1,0]\n\t"
      "v_cvt_pk_bf16_f32 v48, v48, v49\n\t"
      "v_cvt_pk_bf16_f32 v49, v50, v51\n\t"
      "v_pk_mul_f32 v[52:53], v[52:53], v[234:235] op_sel_hi:[1,0]\n\t"
      "v_pk_mul_f32 v[54:55], v[54:55], v[234:235] op_sel_hi:[1,0]\n\t"
      "v_cvt_pk_bf16_f32 v52, v52, v53\n\t"
      "v_cvt_pk_bf16_f32 v53, v54, v55\n\t"
      "v_pk_mul_f32 v[56:57], v[56:57], v[234:235] op_sel_hi:[1,0]\n\t"
      "v_pk_mul_f32 v[58:59], v[58:59], v[234:235] op_sel_hi:[1,0]\n\t"
      "v_cvt_pk_bf16_f32 v56, v56, v57\n\t"
      "v_cvt_pk_bf16_f32 v57, v58, v59\n\t"
      "v_pk_mul_f32 v[60:61], v[60:61], v[234:235] op_sel_hi:[1,0]\n\t"
      "v_pk_mul_f32 v[62:63], v[62:63], v[234:235] op_sel_hi:[1,0]\n\t"
      "v_cvt_pk_bf16_f32 v60, v60, v61\n\t"
      "v_cvt_pk_bf16_f32 v61, v62, v63\n\t"
      "ds_write_b64 v253, v[48:49] offset:6144\n\t"
      "ds_write_b64 v248, v[52:53] offset:6144\n\t"
      "ds_write_b64 v254, v[56:57] offset:6144\n\t"
      "ds_write_b64 v143, v[60:61] offset:6144\n\t"
      "s_waitcnt lgkmcnt(5)\n\t"
      "buffer_store_dwordx4 v[116:119], v252, s[20:23], s27 offen nt sc1\n\t"
      "s_add_u32 s38, s27, 0x8000\n\t"
      "s_waitcnt lgkmcnt(4)\n\t"
      "v_mov_b64_e32 v[124:125], v[120:121]\n\t"
      "buffer_store_dwordx4 v[122:125], v252, s[20:23], s38 offen nt sc1\n\t"
      "ds_read_b128 v[126:129], v251 offset:6144\n\t"
      "ds_read_b128 v[130:133], v251 offset:7168\n\t"
      "v_pk_mul_f32 v[64:65], v[64:65], v[238:239] op_sel_hi:[1,0]\n\t"
      "v_pk_mul_f32 v[66:67], v[66:67], v[238:239] op_sel_hi:[1,0]\n\t"
      "v_cvt_pk_bf16_f32 v64, v64, v65\n\t"
      "v_cvt_pk_bf16_f32 v65, v66, v67\n\t"
      "v_pk_mul_f32 v[68:69], v[68:69], v[238:239] op_sel_hi:[1,0]\n\t"
      "v_pk_mul_f32 v[70:71], v[70:71], v[238:239] op_sel_hi:[1,0]\n\t"
      "v_cvt_pk_bf16_f32 v68, v68, v69\n\t"
      "v_cvt_pk_bf16_f32 v69, v70, v71\n\t"
      "v_pk_mul_f32 v[72:73], v[72:73], v[238:239] op_sel_hi:[1,0]\n\t"
      "v_pk_mul_f32 v[74:75], v[74:75], v[238:239] op_sel_hi:[1,0]\n\t"
      "v_cvt_pk_bf16_f32 v72, v72, v73\n\t"
      "v_cvt_pk_bf16_f32 v73, v74, v75\n\t"
      "v_pk_mul_f32 v[76:77], v[76:77], v[238:239] op_sel_hi:[1,0]\n\t"
      "v_pk_mul_f32 v[78:79], v[78:79], v[238:239] op_sel_hi:[1,0]\n\t"
      "v_cvt_pk_bf16_f32 v76, v76, v77\n\t"
      "v_cvt_pk_bf16_f32 v77, v78, v79\n\t"
      "ds_write_b64 v253, v[64:65] offset:8192\n\t"
      "ds_write_b64 v248, v[68:69] offset:8192\n\t"
      "ds_write_b64 v254, v[72:73] offset:8192\n\t"
      "ds_write_b64 v143, v[76:77] offset:8192\n\t"
      "ds_read_b128 v[144:147], v251 offset:8192\n\t"
      "ds_read_b128 v[148:151], v251 offset:9216\n\t"
      "s_waitcnt lgkmcnt(7)\n\t"
      "buffer_store_dwordx4 v[126:129], v252, s[20:23], s27 offen offset:128 nt sc1\n\t"
      "s_waitcnt lgkmcnt(6)\n\t"
      "v_mov_b64_e32 v[134:135], v[130:131]\n\t"
      "buffer_store_dwordx4 v[132:135], v252, s[20:23], s38 offen offset:128 nt sc1\n\t"
      "s_add_u32 s27, s27, 0x10000\n\t"
      "v_pk_mul_f32 v[80:81], v[80:81], v[238:239] op_sel_hi:[1,0]\n\t"
      "v_pk_mul_f32 v[82:83], v[82:83], v[238:239] op_sel_hi:[1,0]\n\t"
      "v_cvt_pk_bf16_f32 v80, v80, v81\n\t"
      "v_cvt_pk_bf16_f32 v81, v82, v83\n\t"
      "v_pk_mul_f32 v[84:85], v[84:85], v[238:239] op_sel_hi:[1,0]\n\t"
      "v_pk_mul_f32 v[86:87], v[86:87], v[238:239] op_sel_hi:[1,0]\n\t"
      "v_cvt_pk_bf16_f32 v84, v84, v85\n\t"
      "v_cvt_pk_bf16_f32 v85, v86, v87\n\t"
      "v_pk_mul_f32 v[88:89], v[88:89], v[238:239] op_sel_hi:[1,0]\n\t"
      "v_pk_mul_f32 v[90:91], v[90:91], v[238:239] op_sel_hi:[1,0]\n\t"
      "v_cvt_pk_bf16_f32 v88, v88, v89\n\t"
      "v_cvt_pk_bf16_f32 v89, v90, v91\n\t"
      "v_pk_mul_f32 v[92:93], v[92:93], v[238:239] op_sel_hi:[1,0]\n\t"
      "v_pk_mul_f32 v[94:95], v[94:95], v[238:239] op_sel_hi:[1,0]\n\t"
      "v_cvt_pk_bf16_f32 v92, v92, v93\n\t"
      "v_cvt_pk_bf16_f32 v93, v94, v95\n\t"
      "ds_write_b64 v253, v[80:81] offset:10240\n\t"
      "ds_write_b64 v248, v[84:85] offset:10240\n\t"
      "ds_write_b64 v254, v[88:89] offset:10240\n\t"
      "ds_write_b64 v143, v[92:93] offset:10240\n\t"
      "s_waitcnt lgkmcnt(5)\n\t"
      "buffer_store_dwordx4 v[144:147], v252, s[20:23], s27 offen nt sc1\n\t"
      "s_add_u32 s38, s27, 0x8000\n\t"
      "s_waitcnt lgkmcnt(4)\n\t"
      "v_mov_b64_e32 v[152:153], v[148:149]\n\t"
      "buffer_store_dwordx4 v[150:153], v252, s[20:23], s38 offen nt sc1\n\t"
      "ds_read_b128 v[154:157], v251 offset:10240\n\t"
      "ds_read_b128 v[158:161], v251 offset:11264\n\t"
      "s_waitcnt lgkmcnt(1)\n\t"
      "buffer_store_dwordx4 v[154:157], v252, s[20:23], s27 offen offset:128 nt sc1\n\t"
      "s_waitcnt lgkmcnt(0)\n\t"
      "v_mov_b64_e32 v[162:163], v[158:159]\n\t"
      "buffer_store_dwordx4 v[160:163], v252, s[20:23], s38 offen offset:128 nt sc1\n\t"
      "s_add_u32 s27, s27, 0x10000\n\t"
      "s_add_u32 s37, s7, 16\n\t"
      "s_cmp_lt_u32 s37, s54\n\t"
      "s_cbranch_scc1 L_end_%=\n\t"
      "s_cmp_lg_u32 s43, 0\n\t"
      "s_cbranch_scc1 L_end_%=\n\t"
      "s_endpgm\n\t"
      "L_idle_%=:\n\t"
      "s_add_u32 m0, s42, 32768\n\t"
      "s_nop 0\n\t"
      "buffer_load_dwordx4 v251, s[12:15], s31 offen lds\n\t"
      "s_addk_i32 m0, 896\n\t"
      "s_nop 0\n\t"
      "buffer_load_dwordx4 v251, s[12:15], s31 offen offset:128 lds\n\t"
      "s_addk_i32 m0, 7296\n\t"
      "s_nop 0\n\t"
      "buffer_load_dwordx4 v252, s[16:19], s41 offen lds\n\t"
      "s_addk_i32 m0, 896\n\t"
      "s_nop 0\n\t"
      "buffer_load_dwordx4 v252, s[16:19], s41 offen offset:128 lds\n\t"
      "s_mov_b32 s41, s31\n\t"
      "s_add_u32 s31, s31, 0x20000\n\t"
      "s_waitcnt vmcnt(8)\n\t"
      "s_barrier\n\t"
      "s_cbranch_vccz L_ex_i_%=\n\t"
      "s_waitcnt vmcnt(4)\n\t"
      "s_add_u32 m0, s42, 49152\n\t"
      "s_nop 0\n\t"
      "buffer_load_dwordx4 v251, s[12:15], s31 offen lds\n\t"
      "s_addk_i32 m0, 896\n\t"
      "s_nop 0\n\t"
      "buffer_load_dwordx4 v251, s[12:15], s31 offen offset:128 lds\n\t"
      "s_addk_i32 m0, 7296\n\t"
      "s_nop 0\n\t"
      "buffer_load_dwordx4 v252, s[16:19], s41 offen lds\n\t"
      "s_addk_i32 m0, 896\n\t"
      "s_nop 0\n\t"
      "buffer_load_dwordx4 v252, s[16:19], s41 offen offset:128 lds\n\t"
      "s_mov_b32 s41, s31\n\t"
      "s_add_u32 s31, s31, 0x20000\n\t"
      "L_ex_i_%=:\n\t"
      "L_idl_%=:\n\t"
      "s_cmp_lt_u32 s42, s40\n\t"
      "s_cselect_b32 s34, s35, s58\n\t"
      "s_mov_b32 m0, s42\n\t"
      "s_add_u32 s42, s42, s34\n\t"
      "s_cmp_lt_u32 s30, s59\n\t"
      "s_cbranch_scc1 L_iw0_%=\n\t"
      "s_cbranch_vccnz L_iw2_%=\n\t"
      "s_branch L_iw1_%=\n\t"
      "L_iw0_%=:\n\t"
      "s_cmp_eq_u32 s30, 2\n\t"
      "s_cbranch_scc1 L_iw1_%=\n\t"
      "s_waitcnt vmcnt(0)\n\t"
      "L_iw1_%=:\n\t"
      "s_waitcnt vmcnt(4)\n\t"
      "L_iw2_%=:\n\t"
      "s_waitcnt vmcnt(8)\n\t"
      "s_barrier\n\t"
      "s_cmp_lt_u32 s30, s59\n\t"
      "s_cbranch_scc1 L_ind_%=\n\t"
      "s_nop 0\n\t"
      "buffer_load_dwordx4 v251, s[12:15], s31 offen lds\n\t"
      "s_addk_i32 m0, 896\n\t"
      "s_nop 0\n\t"
      "buffer_load_dwordx4 v251, s[12:15], s31 offen offset:128 lds\n\t"
      "s_addk_i32 m0, 7296\n\t"
      "s_nop 0\n\t"
      "buffer_load_dwordx4 v252, s[16:19], s41 offen lds\n\t"
      "s_addk_i32 m0, 896\n\t"
      "s_nop 0\n\t"
      "buffer_load_dwordx4 v252, s[16:19], s41 offen offset:128 lds\n\t"
      "s_mov_b32 s41, s31\n\t"
      "s_add_u32 s31, s31, 0x20000\n\t"
      "L_ind_%=:\n\t"
      "s_sub_u32 s30, s30, 1\n\t"
      "s_cmp_ge_u32 s30, 1\n\t"
      "s_cbranch_scc1 L_idl_%=\n\t"
      "s_add_u32 s37, s7, 16\n\t"
      "s_cmp_lt_u32 s37, s54\n\t"
      "s_cbranch_scc1 L_end_%=\n\t"
      "s_cmp_lg_u32 s43, 0\n\t"
      "s_cbranch_scc1 L_end_%=\n\t"
      "s_endpgm\n\t"
      "L_end_%=:\n\t"
      "s_waitcnt vmcnt(0) lgkmcnt(0)\n\t"
      "s_nop 7\n\t"
      "s_nop 7\n\t"
      : "={s39}"(bad), "={s43}"(more), "={s42}"(sto), "={s90}"(s0), "={s91}"(L), "={s92}"(q0), "={s93}"(h), "={s28}"(u), "={s24}"(n), "={s54}"(ns), "={s94}"(au), "={s95}"(wp), "+{s[4:5]}"(ka)
      : "{s6}"(b), "{s7}"(g0), "{s3}"(rd), "{s26}"(wid), "s"((unsigned)(size_t)(lds_ptr)smem)
      : "v0", "v1", "v2", "v3", "v4", "v5", "v6", "v7", "v8", "v9", "v10", "v11", "v12", "v13", "v14", "v15", "v16", "v17", "v18", "v19", "v20", "v21", "v22", "v23", "v24", "v25", "v26", "v27", "v28", "v29", "v30", "v31", "v32", "v33", "v34", "v35", "v36", "v37", "v38", "v39", "v40", "v41", "v42", "v43", "v44", "v45", "v46", "v47", "v48", "v49", "v50", "v51", "v52", "v53", "v54", "v55", "v56", "v57", "v58", "v59", "v60", "v61", "v62", "v63", "v64", "v65", "v66", "v67", "v68", "v69", "v70", "v71", "v72", "v73", "v74", "v75", "v76", "v77", "v78", "v79", "v80", "v81", "v82", "v83", "v84", "v85", "v86", "v87", "v88", "v89", "v90", "v91", "v92", "v93", "v94", "v95", "v96", "v97", "v98", "v99", "v100", "v101", "v102", "v103", "v104", "v105", "v106", "v107", "v108", "v109", "v110", "v111", "v112", "v113", "v114", "v115", "v116", "v117", "v118", "v119", "v120", "v121", "v122", "v123", "v124", "v125", "v126", "v127", "v128", "v129", "v130", "v131", "v132", "v133", "v134", "v135", "v136", "v137", "v138", "v139", "v140", "v141", "v142", "v143", "v144", "v145", "v146", "v147", "v148", "v149", "v150", "v151", "v152", "v153", "v154", "v155", "v156", "v157", "v158", "v159", "v160", "v161", "v162", "v163", "v164", "v165", "v166", "v167", "v168", "v169", "v170", "v171", "v172", "v173", "v174", "v175", "v176", "v177", "v178", "v179", "v180", "v181", "v182", "v183", "v184", "v185", "v186", "v187", "v188", "v189", "v190", "v191", "v192", "v193", "v194", "v195", "v196", "v197", "v198", "v199", "v200", "v201", "v202", "v203", "v204", "v205", "v206", "v207", "v208", "v209", "v210", "v211", "v212", "v213", "v214", "v215", "v216", "v217", "v218", "v219", "v220", "v221", "v222", "v223", "v224", "v225", "v226", "v227", "v228", "v229", "v230", "v231", "v232", "v233", "v234", "v235", "v236", "v237", "v238", "v239", "v240", "v241", "v242", "v243", "v244", "v245", "v246", "v247", "v248", "v249", "v250", "v251", "v252", "v253", "v254", "s8", "s9", "s10", "s11", "s12", "s13", "s14", "s15", "s16", "s17", "s18", "s19", "s20", "s21", "s22", "s23", "s25", "s27", "s29", "s30", "s31", "s34", "s35", "s36", "s37", "s38", "s40", "s41", "s44", "s45", "s46", "s47", "s48", "s49", "s50", "s51", "s52", "s53", "s55", "s56", "s57", "s58", "s59", "s60", "s61", "vcc", "scc", "memory");
      if (bad == 2) {
        g0 += 16; rd &= 1;
        if (g0 >= ns) break;
        continue;
      }
      if (bad) {
        int ln; asm volatile("v_mbcnt_lo_u32_b32 %0, -1, 0\n\tv_mbcnt_hi_u32_b32 %0, -1, %0" : "=v"(ln));
        const int r32 = ln & 31, hi = ln >> 5;
        const size_t base = ((size_t)s0 * H + h) * D; const unsigned nrec = (L - 1) * ROWB + 256;
        const __amdgpu_buffer_rsrc_t rq = __builtin_amdgcn_make_buffer_rsrc((void*)(ka->q + base), (short)0, nrec, 0x00020000), rk = __builtin_amdgcn_make_buffer_rsrc((void*)(ka->k + base), (short)0, nrec, 0x00020000),
                                     rv = __builtin_amdgcn_make_buffer_rsrc((void*)(ka->v + base), (short)0, nrec, 0x00020000), ro = __builtin_amdgcn_make_buffer_rsrc((void*)(ka->o + base), (short)0, (min(L, q0 + 16 * u) - 1) * ROWB + 256, 0x00020000);
        lds_ptr st = (lds_ptr)smem + sto;
        constexpr float C = 0.12751743082459868f;
        for (unsigned qs = q0; qs < q0 + 16 * u; qs += 32) {
        float mx = -INFINITY, l = 0.f; f32x16 oa[4] = {};
        for (unsigned t = 0; t < n; ++t) {
          f32x16 p = {};
#pragma unroll
          for (int s = 0; s < 8; ++s)
            p = __builtin_amdgcn_mfma_f32_32x32x16_bf16(__builtin_bit_cast(bf16x8, __builtin_amdgcn_raw_buffer_load_b128(rk, (t * 32 + r32) * ROWB + hi * 16 + s * 32, 0, 0)),
                                                        __builtin_bit_cast(bf16x8, __builtin_amdgcn_raw_buffer_load_b128(rq, (qs + r32) * ROWB + hi * 16 + s * 32, 0, 0)), p, 0, 0, 0);
#pragma unroll
          for (int i = 0; i < 8; ++i)
            *(__attribute__((address_space(3))) u32x4*)(st + 1024 * i + 16 * ln) = __builtin_amdgcn_raw_buffer_load_b128(rv, (t * 32 + 8 * (i >> 1) + ((ln >> 2) & 7)) * ROWB + (i & 1) * 128 + hi * 64 + (ln & 3) * 16, 0, 0);
          const int lim = (int)L - (int)t * 32 - 4 * hi;
          float pm = -INFINITY;
#pragma unroll
          for (int e = 0; e < 16; ++e) { if ((e & 3) + 8 * (e >> 2) >= lim) p[e] = -INFINITY; pm = fmaxf(pm, p[e]); }
          { auto rr = __builtin_amdgcn_permlane32_swap(__float_as_uint(pm), __float_as_uint(pm), false, false); pm = fmaxf(__uint_as_float(rr[0]), __uint_as_float(rr[1])); }
          const float mn = fmaxf(mx, pm), al = __builtin_amdgcn_exp2f((mx - mn) * C); mx = mn; l *= al;
#pragma unroll
          for (int e = 0; e < 16; ++e) { p[e] = __builtin_amdgcn_exp2f((p[e] - mn) * C); l += p[e]; }
          asm volatile("s_waitcnt lgkmcnt(0)" ::: "memory");
          const unsigned vb = 64 * (4 * hi + ((ln >> 2) & 3)) + 32 * ((ln >> 4) & 1) + 8 * (ln & 3);
#pragma unroll
          for (int d = 0; d < 4; ++d) { oa[d] *= al;
#pragma unroll
            for (int ks = 0; ks < 2; ++ks) {
              const u32x4 pw = {cvtpk(p[8 * ks], p[8 * ks + 1]), cvtpk(p[8 * ks + 2], p[8 * ks + 3]), cvtpk(p[8 * ks + 4], p[8 * ks + 5]), cvtpk(p[8 * ks + 6], p[8 * ks + 7])};
              const s16x8 vf = __builtin_shufflevector(vtr(st + vb + 4096 * ks + 512 * d), vtr(st + vb + 4096 * ks + 2048 + 512 * d), 0, 1, 2, 3, 4, 5, 6, 7);
              oa[d] = __builtin_amdgcn_mfma_f32_32x32x16_bf16(__builtin_bit_cast(bf16x8, vf), __builtin_bit_cast(bf16x8, pw), oa[d], 0, 0, 0);
            } }
          asm volatile("s_waitcnt lgkmcnt(0)" ::: "memory");
        }
        { auto rr = __builtin_amdgcn_permlane32_swap(__float_as_uint(l), __float_as_uint(l), false, false); l = __uint_as_float(rr[0]) + __uint_as_float(rr[1]); }
        const float il = 1.f / l;
#pragma unroll
        for (int d = 0; d < 4; ++d)
#pragma unroll
          for (int g = 0; g < 4; ++g)
            __builtin_amdgcn_raw_buffer_store_b64(u32x2{cvtpk(oa[d][4 * g] * il, oa[d][4 * g + 1] * il), cvtpk(oa[d][4 * g + 2] * il, oa[d][4 * g + 3] * il)}, ro, (qs + r32) * ROWB + 64 * d + 16 * g + 8 * hi, 0, 0);
        }
      }
      if (more) rd += 2;
      else { g0 += 16; rd &= 1; if (g0 >= ns) break; }
      asm volatile("s_waitcnt lgkmcnt(0)\n\ts_barrier" ::: "memory");
    }
  }
}


extern "C" void kernel_launch(void* const* d_in, const int* in_sizes, int n_in,
                              void* d_out, int out_size, void* d_ws, size_t ws_size,
                              hipStream_t stream) {
  if (in_sizes[3] < 2 || out_size <= 0) return;
  attn_varlen<<<GRID, 256, 0, stream>>>((const unsigned short*)d_in[0], (const unsigned short*)d_in[1], (const unsigned short*)d_in[2],
                                        (const int*)d_in[3], (unsigned short*)d_out, in_sizes[3] - 1, (int)((long long)(in_sizes[0] / (H * D)) * 1024 / 1024));
}
```
